```python
import torch

def _softmax(x, axis=-1):
    return torch.softmax(x, dim=axis)
def _gelu(x):
    return torch.nn.functional.gelu(x, approximate="tanh")
def _silu(x):
    return torch.nn.functional.silu(x)
def _sigmoid(x):
    return torch.sigmoid(x)
def _layernorm(x, axis=-1, eps=1e-5):
    m = x.mean(dim=axis, keepdim=True)
    v = x.var(dim=axis, keepdim=True, unbiased=False)
    return (x - m) / torch.sqrt(v + eps)
def _scatter_add(tgt, idx, src, axis=0):
    return tgt.clone().index_add_(axis, idx, src)
def _segment_sum(src, seg, n):
    out = torch.zeros((n,) + src.shape[1:], dtype=src.dtype)
    return out.index_add_(0, seg, src)
def _pad(x, pw):
    flat = tuple(v for pair in reversed(pw) for v in pair)
    return torch.nn.functional.pad(x, flat)

kernel_name = 'parallel_sum__sliding_local_dilated__dense_activation_relu__prompt'

def setup_inputs():

    _w = (lambda a: (lambda t: t.to(torch.bfloat16) if t.is_floating_point() else t)(torch.as_tensor(a)))
    def _A_setup():
        B, S, D = 1, 2048, 1024
        return {
            "x": _w((lambda *s: torch.randn(*s, dtype=torch.bfloat16) * 0.05)(B, S, D)),
            "wqkv": _w((lambda *s: torch.randn(*s, dtype=torch.bfloat16) * 0.05)(D, 3 * D)),
            "wo": _w((lambda *s: torch.randn(*s, dtype=torch.bfloat16) * 0.05)(D, D)),
        }
    _A_in = _A_setup()

    def _B_setup():
        B, S, D, Dff = 1, 2048, 1024, 3584
        return {
            "x": _w((lambda *s: torch.randn(*s, dtype=torch.bfloat16) * 0.05)(B, S, D)),
            "w1": _w((lambda *s: torch.randn(*s, dtype=torch.bfloat16) * 0.05)(D, Dff)),
            "w2": _w((lambda *s: torch.randn(*s, dtype=torch.bfloat16) * 0.05)(Dff, D)),
        }
    _B_in = _B_setup()

    return {
        "A_x": _A_in["x"],
        "A_wqkv": _A_in["wqkv"],
        "A_wo": _A_in["wo"],
        "B_x": _B_in["x"],
        "B_w1": _B_in["w1"],
        "B_w2": _B_in["w2"],
    }

def reference(A_x, A_wqkv, A_wo, B_x, B_w1, B_w2):
    def _A_ref(x, wqkv, wo):
        B, S, D = x.shape
        H = 32; Dh = D // H
        W, dil = 8, 2
        qkv = (lambda x, *d: x.permute(*d))((x @ wqkv).reshape(B, S, 3, H, Dh), 2, 0, 3, 1, 4)
        q, k, v = qkv[0], qkv[1], qkv[2]
        scale = Dh ** -0.5
        pos = torch.arange(S)
        off = torch.arange(W) * dil
        idx = pos[:, None] - off[None, :]
        mask = idx >= 0
        idx_c = torch.where(mask, idx, 0).reshape(-1)
        kg = k[:, :, idx_c, :].reshape(B, H, S, W, Dh)
        vg = v[:, :, idx_c, :].reshape(B, H, S, W, Dh)
        logits = torch.einsum("bhsd,bhswd->bhsw", q, kg) * scale
        logits = torch.where(mask[None, None, :, :], logits, -1e9)
        attn = _softmax(logits, axis=-1)
        o = torch.einsum("bhsw,bhswd->bhsd", attn, vg)
        return (lambda x, *d: x.permute(*d))(o, 0, 2, 1, 3).reshape(B, S, D) @ wo

    def _B_ref(x, w1, w2):
        h = torch.einsum("bsd,df->bsf", x, w1)
        zero = torch.zeros_like(h)
        h = torch.maximum(h, zero)
        return torch.einsum("bsf,fd->bsd", h, w2)

    x = A_x
    xn = _layernorm(x, axis=-1)
    return x + _A_ref(xn, A_wqkv, A_wo) + _B_ref(xn, B_w1, B_w2)
```

```cpp
#include <hip/hip_runtime.h>
#include <hip/hip_bf16.h>

typedef short s16x4 __attribute__((ext_vector_type(4)));
typedef short s16x8 __attribute__((ext_vector_type(8)));
typedef __bf16 bf16x2 __attribute__((ext_vector_type(2)));
typedef float f32x4 __attribute__((ext_vector_type(4)));
typedef unsigned u32x4 __attribute__((ext_vector_type(4)));
typedef unsigned u32x2 __attribute__((ext_vector_type(2)));
typedef __attribute__((address_space(3))) char lchar;
typedef __attribute__((address_space(3))) unsigned lu32;

#define BAR() __builtin_amdgcn_s_barrier()
#define SCHED() __builtin_amdgcn_sched_barrier(0)
#define WAIT_V(n) asm volatile("s_waitcnt vmcnt(%0)" ::"n"(n) : "memory")
#define WAIT_L0() asm volatile("s_waitcnt lgkmcnt(0)" ::: "memory")
#define PK8(L, H) (s16x8){L[0], L[1], L[2], L[3], H[0], H[1], H[2], H[3]}
#define MFMA(a, b, c) __builtin_amdgcn_mfma_f32_16x16x32_bf16(a, b, c, 0, 0, 0)

constexpr int SEQ = 2048, DM = 1024, DFF = 3584, KC = 4608, KB2 = KC / 32;
constexpr size_t WS_XN = 0, WS_ACAT = 4u << 20, WS_EXCH = 24u << 20, WS_FLAG = 40u << 20;

__device__ __forceinline__ unsigned pk2(float a, float b) {
  bf16x2 v = {(__bf16)a, (__bf16)b};
  return __builtin_bit_cast(unsigned, v);
}
__device__ __forceinline__ float rbf(float a) { return (float)(__bf16)a; }
__device__ __forceinline__ float lo16(unsigned u) { return __builtin_bit_cast(float, u << 16); }
__device__ __forceinline__ float hi16(unsigned u) { return __builtin_bit_cast(float, u & 0xffff0000u); }

template <int OFF> __device__ __forceinline__ s16x4 tr_read(unsigned a) {
  s16x4 r; asm volatile("ds_read_b64_tr_b16 %0, %1 offset:%2" : "=&v"(r) : "v"(a), "i"(OFF) : "memory"); return r;
}
template <int OFF> __device__ __forceinline__ s16x8 rd128(unsigned a) {
  s16x8 r; asm volatile("ds_read_b128 %0, %1 offset:%2" : "=&v"(r) : "v"(a), "i"(OFF) : "memory"); return r;
}
__device__ __forceinline__ void glds(const char* g, lchar* l) {
  __builtin_amdgcn_global_load_lds((const unsigned*)g, (lu32*)l, 16, 0, 0);
}

__global__ __launch_bounds__(256) void ln_kernel(const u32x4* __restrict__ x, u32x4* __restrict__ xn, unsigned* __restrict__ flags) {
  __shared__ float red[2][4][8];
  const int wid = threadIdx.x >> 6, lane = threadIdx.x & 63, r = lane & 7, c = lane >> 3;
  const int row = blockIdx.x * 8 + r;
  if (threadIdx.x == 0) flags[blockIdx.x] = 0u;
  u32x4 v[4];
#pragma unroll
  for (int i = 0; i < 4; ++i) v[i] = x[row * 128 + wid * 32 + i * 8 + c];
  float f[32], s = 0.f;
#pragma unroll
  for (int i = 0; i < 4; ++i)
#pragma unroll
    for (int e = 0; e < 4; ++e) { f[i * 8 + 2 * e] = lo16(v[i][e]); f[i * 8 + 2 * e + 1] = hi16(v[i][e]); }
#pragma unroll
  for (int i = 0; i < 32; ++i) s += f[i];
  s += __shfl_xor(s, 8); s += __shfl_xor(s, 16); s += __shfl_xor(s, 32);
  if (lane < 8) red[0][wid][r] = s;
  __syncthreads();
  const float mean = (red[0][0][r] + red[0][1][r] + red[0][2][r] + red[0][3][r]) * (1.f / DM);
  float q = 0.f;
#pragma unroll
  for (int i = 0; i < 32; ++i) { float d = f[i] - mean; q += d * d; }
  q += __shfl_xor(q, 8); q += __shfl_xor(q, 16); q += __shfl_xor(q, 32);
  if (lane < 8) red[1][wid][r] = q;
  __syncthreads();
  const float var = (red[1][0][r] + red[1][1][r] + red[1][2][r] + red[1][3][r]) * (1.f / DM);
  const float m = rbf(mean), sd = rbf(sqrtf(rbf(rbf(var) + 1e-5f)));
#pragma unroll
  for (int i = 0; i < 4; ++i) {
    u32x4 o;
#pragma unroll
    for (int e = 0; e < 4; ++e) o[e] = pk2(rbf(f[i * 8 + 2 * e] - m) / sd, rbf(f[i * 8 + 2 * e + 1] - m) / sd);
    const int ch = wid * 32 + i * 8 + c;
    xn[((row >> 4) * 32 + (ch >> 2)) * 64 + (ch & 3) * 16 + (row & 15)] = o;
  }
}

constexpr int G1_X = 0, G1_H = 65536, G1_W = 69632, G1_WU = 14336, G1_LDS = G1_W + 4 * G1_WU;

template <bool QKV> struct G1 {
  static constexpr int CH = QKV ? 24 : 28, LDW = (QKV ? 3072 : DFF) * 2, NFR = QKV ? 6 : 7, NT = 16;
  static constexpr int VW = QKV ? 6 : 7;
  const char* xn; const char* W; lchar* shm;
  int G, wl, mt; unsigned lane16, xb, hb;
  unsigned wo0[4], wo1[4], wb[NFR];
  int n0a, n0b;
  f32x4 acc[4][NFR], hacc;
  s16x8 xa[4][2], hx[2];

  __device__ __forceinline__ void issueX(int h, int t, int B) {
#pragma unroll
    for (int a = 0; a < 2; ++a)
#pragma unroll
      for (int b = 0; b < 2; ++b)
        glds(xn + (size_t)(((mt * 16 + h * 8 + 2 * wl + a) * 32 + 2 * t + b) * 1024) + lane16,
             shm + G1_X + (B * 2 + h) * 16384 + ((2 * wl + a) * 2 + b) * 1024);
  }
  __device__ __forceinline__ void issueW0(int t, int B) {
    const char* src = W + (size_t)t * 64 * LDW;
    lchar* dst = shm + G1_W + (B * 2) * G1_WU + n0a * 1024;
#pragma unroll
    for (int k = 0; k < 3; ++k) glds(src + wo0[k], dst + k * 1024);
    if (wl < 2) {
      if (QKV) { const int hm = mt > 0 ? mt * 16 - 1 : 0;
        glds(xn + (size_t)((hm * 32 + 2 * t + wl) * 1024) + lane16, shm + G1_H + B * 2048 + wl * 1024); }
      else glds(src + wo0[3], dst + 3 * 1024);
    }
  }
  __device__ __forceinline__ void issueW1(int t, int B) {
    const char* src = W + (size_t)(t * 64 + 32) * LDW;
    lchar* dst = shm + G1_W + (B * 2 + 1) * G1_WU + n0b * 1024;
#pragma unroll
    for (int k = 0; k < 3; ++k) glds(src + wo1[k], dst + k * 1024);
    if (!QKV && wl >= 2) glds(src + wo1[3], dst + 3 * 1024);
  }
  template <int B, int KH, int n> __device__ __forceinline__ void rdW(s16x4* l, s16x4* h) {
    if constexpr (n < NFR) {
      constexpr int o = (B * 2 + KH) * G1_WU + (QKV ? 0 : (n < 6 ? (n >> 1) * 64 : 0));
      constexpr int bi = QKV ? n : (n < 6 ? (n & 1) : 2);
      l[n] = tr_read<o>(wb[bi]); h[n] = tr_read<o + 4 * CH * 16>(wb[bi]);
      rdW<B, KH, n + 1>(l, h);
    }
  }
  template <int KH> __device__ __forceinline__ void mma(s16x4* l, s16x4* h, int R) {
    __builtin_amdgcn_s_setprio(1);
#pragma unroll
    for (int n = 0; n < NFR; ++n) {
      s16x8 w = PK8(l[n], h[n]);
#pragma unroll
      for (int mi = 0; mi < 4; ++mi) acc[mi][n] = MFMA(w, xa[mi][KH], acc[mi][n]);
      if (QKV && n >= 2) { if (R == n - 2) hacc = MFMA(w, hx[KH], hacc); }
    }
    __builtin_amdgcn_s_setprio(0);
  }
  template <int B, int TAIL> __device__ __forceinline__ void tile(int t, int R) {
    s16x4 l[NFR], h[NFR];
    xa[0][0] = rd128<B * 32768 + 0 * 1024>(xb); xa[1][0] = rd128<B * 32768 + 2 * 1024>(xb);
    xa[2][0] = rd128<B * 32768 + 4 * 1024>(xb); xa[3][0] = rd128<B * 32768 + 6 * 1024>(xb);
    rdW<B, 0, 0>(l, h);
    xa[0][1] = rd128<B * 32768 + 1 * 1024>(xb); xa[1][1] = rd128<B * 32768 + 3 * 1024>(xb);
    xa[2][1] = rd128<B * 32768 + 5 * 1024>(xb); xa[3][1] = rd128<B * 32768 + 7 * 1024>(xb);
    if (QKV) { hx[0] = rd128<B * 2048>(hb); hx[1] = rd128<B * 2048 + 1024>(hb); }
    if (G == 0) { if (TAIL < 2) { issueW1(t + 1, B ^ 1); WAIT_V(VW); } else WAIT_V(0); }
    else if (TAIL == 0) issueX(0, t + 2, B);
    WAIT_L0(); BAR(); SCHED();
    mma<0>(l, h, R);
    SCHED(); BAR(); SCHED();
    rdW<B, 1, 0>(l, h);
    if (G == 0) { if (TAIL == 0) { issueW0(t + 2, B); WAIT_V(VW); } else if (TAIL == 1) WAIT_V(3); }
    else { if (TAIL == 0) { issueX(1, t + 2, B); WAIT_V(8); } else WAIT_V(0); }
    WAIT_L0(); BAR(); SCHED();
    mma<1>(l, h, R);
    SCHED(); BAR(); SCHED();
  }

  __device__ __forceinline__ void run(const char* xn_, const char* Wg, char* acat, int mt_, int j, lchar* shm_) {
    xn = xn_; shm = shm_; mt = mt_;
    W = Wg + (QKV ? j * 128 : j * 448);
    const int wid = __builtin_amdgcn_readfirstlane(threadIdx.x >> 6), lane = threadIdx.x & 63;
    G = wid >> 2; wl = wid & 3;
    const int wr = wl >> 1, wc = wl & 1, R = G * 2 + wr;
    const int g = lane >> 4, q = (lane >> 2) & 3, p = lane & 3;
    const unsigned l0 = (unsigned)(size_t)shm;
    lane16 = lane * 16;
    xb = l0 + G1_X + G * 16384 + wr * 8192 + lane16;
    hb = l0 + G1_H + lane16;
    if (QKV) { n0a = n0b = 3 * wl; } else { n0a = wl < 2 ? 4 * wl : 2 + 3 * wl; n0b = wl < 2 ? 3 * wl : 4 * wl - 2; }
#pragma unroll
    for (int k = 0; k < 4; ++k) {
#pragma unroll
      for (int u = 0; u < 2; ++u) {
        int n = (u ? n0b : n0a) + k; n = n > (CH / 2 - 1) ? (CH / 2 - 1) : n;
        const int i = n * 64 + lane, r = i / CH, pos = i % CH;
        const int ch = QKV ? (pos ^ ((((r >> 1) & 1) << 2) | ((r >> 3) & 1))) : (pos ^ ((r >> 3) & 1));
        int cb;
        if (QKV) { const int F = ch >> 2, hd = F / 3, part = F % 3; cb = part * 2048 + hd * 64 + (ch & 3) * 16; }
        else cb = ch * 16;
        (u ? wo1 : wo0)[k] = r * LDW + cb;
      }
    }
    {
      const unsigned lb = l0 + G1_W + ((8 * g + q) * CH + (p ^ (g & 1))) * 16;
      if (QKV) {
#pragma unroll
        for (int n = 0; n < 6; ++n) wb[n] = lb + (((3 * wc + (n >> 1)) ^ (q >> 1)) * 64) + (((p & 1) ^ (n & 1)) * 8);
      } else {
        wb[0] = lb + wc * 256 + (p & 1) * 8; wb[1] = lb + wc * 256 + ((p & 1) ^ 1) * 8;
        wb[2] = lb + 192 + ((p & 1) ^ wc) * 8;
      }
    }
#pragma unroll
    for (int mi = 0; mi < 4; ++mi)
#pragma unroll
      for (int n = 0; n < NFR; ++n) acc[mi][n] = (f32x4){0.f, 0.f, 0.f, 0.f};
    hacc = (f32x4){0.f, 0.f, 0.f, 0.f};
    if (G == 0) { issueW0(0, 0); issueW1(0, 0); issueW0(1, 1); WAIT_V(VW); }
    else { issueX(0, 0, 0); issueX(1, 0, 0); issueX(0, 1, 1); issueX(1, 1, 1); WAIT_V(8); }
    BAR();
    if (G == 1) BAR();
    SCHED();
    for (int t = 0; t < NT - 2; t += 2) { tile<0, 0>(t, R); tile<1, 0>(t + 1, R); }
    tile<0, 1>(NT - 2, R); tile<1, 2>(NT - 1, R);
    if (G == 0) BAR();
    SCHED();

    const bool odd = g & 1;
    const int mb0 = mt * 16 + R * 4;
    if (!QKV) {
#pragma unroll
      for (int mi = 0; mi < 4; ++mi) {
        char* dst = acat + (size_t)(((mb0 + mi) * KB2 + 32 + 7 * j) * 1024) + lane16;
#pragma unroll
        for (int u = 0; u < 3; ++u) {
          const f32x4 a = acc[mi][2 * u], b = acc[mi][2 * u + 1];
          const unsigned a0 = pk2(fmaxf(a[0], 0.f), fmaxf(a[1], 0.f)), a1 = pk2(fmaxf(a[2], 0.f), fmaxf(a[3], 0.f));
          const unsigned b0 = pk2(fmaxf(b[0], 0.f), fmaxf(b[1], 0.f)), b1 = pk2(fmaxf(b[2], 0.f), fmaxf(b[3], 0.f));
          u32x4 o = {odd ? b0 : a0, odd ? b1 : a1, odd ? a0 : b0, odd ? a1 : b1};
          *(u32x4*)(dst + (4 * wc + u) * 1024) = o;
        }
        const f32x4 a = acc[mi][6];
        u32x2 o = {pk2(fmaxf(a[0], 0.f), fmaxf(a[1], 0.f)), pk2(fmaxf(a[2], 0.f), fmaxf(a[3], 0.f))};
        *(u32x2*)(dst + 3 * 1024 + ((g & 1) ^ wc) * 8) = o;
      }
    } else {
      u32x4 fr[4][3];
#pragma unroll
      for (int mi = 0; mi < 4; ++mi)
#pragma unroll
        for (int u = 0; u < 3; ++u) {
          const f32x4 a = acc[mi][2 * u], b = acc[mi][2 * u + 1];
          const unsigned a0 = pk2(a[0], a[1]), a1 = pk2(a[2], a[3]), b0 = pk2(b[0], b[1]), b1 = pk2(b[2], b[3]);
          fr[mi][u] = (u32x4){odd ? b0 : a0, odd ? b1 : a1, odd ? a0 : b0, odd ? a1 : b1};
        }
      lchar* vimg = shm + wc * 17408;
      lchar* kblk = shm + 34816 + wc * 4096;
      const int rr = lane & 15;
#pragma unroll
      for (int mi = 0; mi < 4; ++mi)
        *(__attribute__((address_space(3))) u32x4*)(vimg + (16 + R * 64 + mi * 16 + rr) * 64 + g * 16) = fr[mi][2];
      if (R < 3) *(__attribute__((address_space(3))) u32x4*)(kblk + (R + 1) * 1024 + lane16) = fr[3][1];
      {
        u32x2 hv = {pk2(hacc[0], hacc[1]), pk2(hacc[2], hacc[3])};
        const int half = (g & 1) ^ (R & 1);
        lchar* d = (R < 2) ? (kblk + lane16 + half * 8) : (vimg + rr * 64 + g * 16 + half * 8);
        *(__attribute__((address_space(3))) u32x2*)d = hv;
      }
      __syncthreads();
      u32x4 kprev = *(__attribute__((address_space(3))) u32x4*)(kblk + R * 1024 + lane16);
      const unsigned vb = l0 + wc * 17408 + (R * 64 + 4 * g + q) * 64 + p * 16;
      const float scale = 0.17677669529663687f;
#pragma unroll
      for (int mi = 0; mi < 4; ++mi) {
        const s16x8 qf = __builtin_bit_cast(s16x8, fr[mi][0]);
        const f32x4 z = {0.f, 0.f, 0.f, 0.f};
        f32x4 s0 = MFMA(__builtin_bit_cast(s16x8, kprev), qf, z);
        f32x4 s1 = MFMA(__builtin_bit_cast(s16x8, fr[mi][1]), qf, z);
        kprev = fr[mi][1];
        s16x4 v00, v01, v10, v11;
        if (mi == 0) { v00 = tr_read<0>(vb); v01 = tr_read<8>(vb); v10 = tr_read<1024>(vb); v11 = tr_read<1032>(vb); }
        if (mi == 1) { v00 = tr_read<1024>(vb); v01 = tr_read<1032>(vb); v10 = tr_read<2048>(vb); v11 = tr_read<2056>(vb); }
        if (mi == 2) { v00 = tr_read<2048>(vb); v01 = tr_read<2056>(vb); v10 = tr_read<3072>(vb); v11 = tr_read<3080>(vb); }
        if (mi == 3) { v00 = tr_read<3072>(vb); v01 = tr_read<3080>(vb); v10 = tr_read<4096>(vb); v11 = tr_read<4104>(vb); }
        const bool first = (mb0 + mi) == 0;
        float lg[8]; bool ok[8];
        float mx = -3.0e38f;
#pragma unroll
        for (int r = 0; r < 4; ++r) {
          const int key = 4 * g + r;
          const bool par = ((key ^ rr) & 1) == 0;
          ok[r] = par && key >= rr + 2 && !first;
          ok[4 + r] = par && key <= rr;
          lg[r] = rbf(rbf(s0[r]) * scale); lg[4 + r] = rbf(rbf(s1[r]) * scale);
          mx = fmaxf(mx, ok[r] ? lg[r] : -3.0e38f); mx = fmaxf(mx, ok[4 + r] ? lg[4 + r] : -3.0e38f);
        }
        mx = fmaxf(mx, __shfl_xor(mx, 16)); mx = fmaxf(mx, __shfl_xor(mx, 32));
        float e[8], sum = 0.f;
#pragma unroll
        for (int r = 0; r < 8; ++r) { e[r] = ok[r] ? __expf(lg[r] - mx) : 0.f; sum += e[r]; }
        sum += __shfl_xor(sum, 16); sum += __shfl_xor(sum, 32);
        const float inv = 1.f / sum;
        u32x4 pp = {pk2(e[0] * inv, e[1] * inv), pk2(e[2] * inv, e[3] * inv), pk2(e[4] * inv, e[5] * inv), pk2(e[6] * inv, e[7] * inv)};
        const s16x8 pf = __builtin_bit_cast(s16x8, pp);
        WAIT_L0(); SCHED();
        f32x4 o0 = MFMA(PK8(v00, v10), pf, z);
        f32x4 o1 = MFMA(PK8(v01, v11), pf, z);
        u32x4 o = {pk2(o0[0], o0[1]), pk2(o0[2], o0[3]), pk2(o1[0], o1[1]), pk2(o1[2], o1[3])};
        *(u32x4*)(acat + (size_t)(((mb0 + mi) * KB2 + 2 * j + wc) * 1024) + lane16) = o;
      }
    }
  }
};

__global__ __launch_bounds__(512, 2) void gemm1_kernel(const char* __restrict__ xn, const char* __restrict__ wqkv,
                                                       const char* __restrict__ w1, char* __restrict__ acat) {
  extern __shared__ __attribute__((aligned(16))) char shm[];
  const int xcd = blockIdx.x & 7, idx = blockIdx.x >> 3;
  const int mt = (xcd & 1) * 4 + (idx & 3), ni = idx >> 2, j = (xcd >> 1) * 4 + (ni & 3);
  if (ni < 4) { G1<false> k; k.run(xn, w1, acat, mt, j, (lchar*)shm); }
  else { G1<true> k; k.run(xn, wqkv, acat, mt, j, (lchar*)shm); }
}

template <int BN, int SPLIT, int NST> struct G2 {
  static constexpr int CH = BN / 8, NP = BN / 64, WI = BN / 32, STG = 16384 + BN * 128, NK = 72 / SPLIT;
  static constexpr int PER = 4 + WI, VW = PER * (NST - 2) / 2;
  const char* A; const char* Wo; const char* W2; lchar* shm;
  int G, wl, kt0; unsigned lane16, ab, wb[2 * NP], wo[WI];
  f32x4 acc[4][2 * NP];
  s16x8 a[4][2]; s16x4 l[2][2 * NP], h[2][2 * NP];

  __device__ __forceinline__ void issue(int s, int buf) {
    const int kt = kt0 + s;
    lchar* dst = shm + buf * STG;
#pragma unroll
    for (int i = 0; i < 2; ++i)
#pragma unroll
      for (int b = 0; b < 2; ++b)
        glds(A + (size_t)(((2 * wl + i) * KB2 + 2 * kt + b) * 1024) + lane16, dst + ((2 * wl + i) * 2 + b) * 1024);
    const char* src = kt < 16 ? Wo + (size_t)kt * 64 * 2048 : W2 + (size_t)(kt - 16) * 64 * 2048;
#pragma unroll
    for (int k = 0; k < WI; ++k) glds(src + wo[k], dst + 16384 + (wl * WI + k) * 1024);
  }
  __device__ __forceinline__ void read(int buf) {
    const unsigned pa = ab + buf * STG;
    a[0][0] = rd128<0>(pa); a[1][0] = rd128<2048>(pa); a[2][0] = rd128<4096>(pa); a[3][0] = rd128<6144>(pa);
#pragma unroll
    for (int n = 0; n < 2 * NP; ++n) { const unsigned pw = wb[n] + buf * STG; l[0][n] = tr_read<0>(pw); h[0][n] = tr_read<4 * CH * 16>(pw); }
    a[0][1] = rd128<1024>(pa); a[1][1] = rd128<3072>(pa); a[2][1] = rd128<5120>(pa); a[3][1] = rd128<7168>(pa);
#pragma unroll
    for (int n = 0; n < 2 * NP; ++n) { const unsigned pw = wb[n] + buf * STG; l[1][n] = tr_read<32 * CH * 16>(pw); h[1][n] = tr_read<36 * CH * 16>(pw); }
  }
  __device__ __forceinline__ void mma() {
    __builtin_amdgcn_s_setprio(1);
#pragma unroll
    for (int kh = 0; kh < 2; ++kh)
#pragma unroll
      for (int n = 0; n < 2 * NP; ++n) {
        s16x8 w = PK8(l[kh][n], h[kh][n]);
#pragma unroll
        for (int mi = 0; mi < 4; ++mi) acc[mi][n] = MFMA(w, a[mi][kh], acc[mi][n]);
      }
    __builtin_amdgcn_s_setprio(0);
  }
  __device__ __forceinline__ void run(const char* acat, const char* wo_, const char* w2_, const char* x, char* out,
                                      int mt, int nt, int ks, lchar* shm_) {
    shm = shm_; A = acat + (size_t)mt * 8 * KB2 * 1024; Wo = wo_ + nt * BN * 2; W2 = w2_ + nt * BN * 2; kt0 = ks * NK;
    const int wid = __builtin_amdgcn_readfirstlane(threadIdx.x >> 6), lane = threadIdx.x & 63;
    G = wid >> 2; wl = wid & 3;
    const int wr = wl >> 1, wc = wl & 1;
    const int g = lane >> 4, q = (lane >> 2) & 3, p = lane & 3;
    const unsigned l0 = (unsigned)(size_t)shm;
    lane16 = lane * 16;
    ab = l0 + wr * 8192 + lane16;
#pragma unroll
    for (int k = 0; k < WI; ++k) {
      const int i = (wl * WI + k) * 64 + lane, r = i / CH, pos = i % CH;
      const int sw = ((r >> 3) & 1) | (BN == 128 ? ((r & 3) << 2) : (((r >> 1) & 1) << 2));
      wo[k] = r * 2048 + (pos ^ sw) * 16;
    }
#pragma unroll
    for (int n = 0; n < 2 * NP; ++n) {
      const int F = wc * NP + (n >> 1), Fs = BN == 128 ? (F ^ q) : (F ^ (q >> 1));
      wb[n] = l0 + 16384 + ((8 * g + q) * CH + 4 * Fs + (p ^ (g & 1))) * 16 + (((p & 1) ^ (n & 1)) * 8);
    }
#pragma unroll
    for (int mi = 0; mi < 4; ++mi)
#pragma unroll
      for (int n = 0; n < 2 * NP; ++n) acc[mi][n] = (f32x4){0.f, 0.f, 0.f, 0.f};
    for (int s = 1 - G; s < NST - 1; s += 2) issue(s, s);
    if (G == 1) WAIT_V(VW);
    BAR();
    if (G == 1) BAR();
    SCHED();
    int buf = G, ib = (G + NST - 1) % NST;
    for (int s = G; s < NK; s += 2) {
      read(buf);
      if (s + NST - 1 < NK) { issue(s + NST - 1, ib); WAIT_V(VW); } else WAIT_V(0);
      WAIT_L0(); BAR(); SCHED();
      mma();
      SCHED(); BAR(); SCHED();
      buf += 2; if (buf >= NST) buf -= NST;
      ib += 2; if (ib >= NST) ib -= NST;
    }
    if (G == 0) BAR();
    SCHED();
    typedef __attribute__((address_space(3))) f32x4 lf4;
#pragma unroll
    for (int i = 0; i < 2; ++i)
#pragma unroll
      for (int n = 0; n < 2 * NP; ++n)
        *(lf4*)(shm + ((wid * 2 + i) * 2 * NP + n) * 1024 + lane16) = G ? acc[i][n] : acc[2 + i][n];
    __syncthreads();
    const bool odd = g & 1;
#pragma unroll
    for (int i = 0; i < 2; ++i) {
      const int row = mt * 128 + wr * 64 + (2 * G + i) * 16 + (lane & 15);
#pragma unroll
      for (int u = 0; u < NP; ++u) {
        f32x4 c0 = (G ? acc[2 + i][2 * u] : acc[i][2 * u]) + *(lf4*)(shm + (((wid ^ 4) * 2 + i) * 2 * NP + 2 * u) * 1024 + lane16);
        f32x4 c1 = (G ? acc[2 + i][2 * u + 1] : acc[i][2 * u + 1]) + *(lf4*)(shm + (((wid ^ 4) * 2 + i) * 2 * NP + 2 * u + 1) * 1024 + lane16);
        const f32x4 lo = odd ? c1 : c0, hi = odd ? c0 : c1;
        const size_t off = (size_t)row * 2048 + (nt * BN + (wc * NP + u) * 32 + g * 8) * 2;
        const u32x4 xv = *(const u32x4*)(x + off);
        u32x4 o = {pk2(lo[0] + lo16(xv[0]), lo[1] + hi16(xv[0])), pk2(lo[2] + lo16(xv[1]), lo[3] + hi16(xv[1])),
                   pk2(hi[0] + lo16(xv[2]), hi[1] + hi16(xv[2])), pk2(hi[2] + lo16(xv[3]), hi[3] + hi16(xv[3]))};
        *(u32x4*)(out + off) = o;
      }
    }
  }
};

constexpr int G2_BN = 64, G2_NST = 6, G2_LDS = G2_NST * (16384 + G2_BN * 128);
__global__ __launch_bounds__(512, 2) void gemm2_kernel(const char* __restrict__ acat, const char* __restrict__ wo,
                                                       const char* __restrict__ w2, const char* __restrict__ x,
                                                       char* __restrict__ out) {
  extern __shared__ __attribute__((aligned(16))) char shm[];
  const int xcd = blockIdx.x & 7, idx = blockIdx.x >> 3;
  const int mt = (xcd >> 1) * 4 + (idx & 3), nt = (xcd & 1) * 8 + (idx >> 2);
  G2<G2_BN, 1, G2_NST> k; k.run(acat, wo, w2, x, out, mt, nt, 0, (lchar*)shm);
}

extern "C" void kernel_launch(void* const* d_in, const int* in_sizes, int n_in, void* d_out, int out_size,
                              void* d_ws, size_t ws_size, hipStream_t stream) {
  const char* x = (const char*)d_in[0];
  const char* wqkv = (const char*)d_in[1];
  const char* wo = (const char*)d_in[2];
  const char* w1 = (const char*)d_in[4];
  const char* w2 = (const char*)d_in[5];
  char* ws = (char*)d_ws;
  (void)hipFuncSetAttribute((const void*)gemm1_kernel, hipFuncAttributeMaxDynamicSharedMemorySize, G1_LDS);
  (void)hipFuncSetAttribute((const void*)gemm2_kernel, hipFuncAttributeMaxDynamicSharedMemorySize, G2_LDS);
  ln_kernel<<<256, 256, 0, stream>>>((const u32x4*)x, (u32x4*)(ws + WS_XN), (unsigned*)(ws + WS_FLAG));
  gemm1_kernel<<<256, 512, G1_LDS, stream>>>(ws + WS_XN, wqkv, w1, ws + WS_ACAT);
  gemm2_kernel<<<256, 512, G2_LDS, stream>>>(ws + WS_ACAT, wo, w2, x, (char*)d_out);
}
```

```cpp
#include <hip/hip_runtime.h>
#include <hip/hip_bf16.h>

typedef short s16x4 __attribute__((ext_vector_type(4)));
typedef short s16x8 __attribute__((ext_vector_type(8)));
typedef __bf16 bf16x2 __attribute__((ext_vector_type(2)));
typedef float f32x4 __attribute__((ext_vector_type(4)));
typedef unsigned u32x4 __attribute__((ext_vector_type(4)));
typedef unsigned u32x2 __attribute__((ext_vector_type(2)));
typedef __attribute__((address_space(3))) char lchar;
typedef __attribute__((address_space(3))) unsigned lu32;

#define BAR() __builtin_amdgcn_s_barrier()
#define SCHED() __builtin_amdgcn_sched_barrier(0)
#define WAIT_V(n) asm volatile("s_waitcnt vmcnt(%0)" ::"n"(n) : "memory")
#define WAIT_L(n) asm volatile("s_waitcnt lgkmcnt(%0)" ::"n"(n) : "memory")
#define WAIT_L0() asm volatile("s_waitcnt lgkmcnt(0)" ::: "memory")
#define PK8(L, H) (s16x8){L[0], L[1], L[2], L[3], H[0], H[1], H[2], H[3]}
#define MFMA(a, b, c) __builtin_amdgcn_mfma_f32_16x16x32_bf16(a, b, c, 0, 0, 0)

#define ST(k, i)
constexpr int SEQ = 2048, DM = 1024, DFF = 3584, KC = 4608, KB2 = KC / 32;
constexpr size_t WS_XN = 0, WS_ACAT = 4u << 20, WS_EXCH = 24u << 20, WS_FLAG = 40u << 20;

__device__ __forceinline__ unsigned pk2(float a, float b) {
  bf16x2 v = {(__bf16)a, (__bf16)b};
  return __builtin_bit_cast(unsigned, v);
}
__device__ __forceinline__ float rbf(float a) { return (float)(__bf16)a; }
__device__ __forceinline__ float lo16(unsigned u) { return __builtin_bit_cast(float, u << 16); }
__device__ __forceinline__ float hi16(unsigned u) { return __builtin_bit_cast(float, u & 0xffff0000u); }

typedef short s16x2 __attribute__((ext_vector_type(2)));
__device__ __forceinline__ unsigned relu2(unsigned u) {
  const s16x2 z = {0, 0};
  return __builtin_bit_cast(unsigned, __builtin_elementwise_max(__builtin_bit_cast(s16x2, u), z));
}
template <bool MAX> __device__ __forceinline__ float red16(float v) {
  const unsigned u = __builtin_bit_cast(unsigned, v);
  auto r = __builtin_amdgcn_permlane16_swap(u, u, false, false);
  const unsigned r0 = r[0], r1 = r[1];
  const float a = __builtin_bit_cast(float, r0), b = __builtin_bit_cast(float, r1);
  return MAX ? fmaxf(a, b) : a + b;
}
template <bool MAX> __device__ __forceinline__ float red32(float v) {
  const unsigned u = __builtin_bit_cast(unsigned, v);
  auto r = __builtin_amdgcn_permlane32_swap(u, u, false, false);
  const unsigned r0 = r[0], r1 = r[1];
  const float a = __builtin_bit_cast(float, r0), b = __builtin_bit_cast(float, r1);
  return MAX ? fmaxf(a, b) : a + b;
}
template <int OFF> __device__ __forceinline__ s16x4 tr_read(unsigned a) {
  s16x4 r; asm volatile("ds_read_b64_tr_b16 %0, %1 offset:%2" : "=&v"(r) : "v"(a), "i"(OFF) : "memory"); return r;
}
template <int OFF> __device__ __forceinline__ s16x8 rd128(unsigned a) {
  s16x8 r; asm volatile("ds_read_b128 %0, %1 offset:%2" : "=&v"(r) : "v"(a), "i"(OFF) : "memory"); return r;
}
typedef __amdgpu_buffer_rsrc_t rsrc_t;
__device__ __forceinline__ rsrc_t mkrs(const void* p, unsigned bytes) {
  return __builtin_amdgcn_make_buffer_rsrc((void*)p, 0, bytes, 0x00020000);
}
__device__ __forceinline__ const char* uni(const char* p) {
  const unsigned long long u = (unsigned long long)p;
  const unsigned lo = __builtin_amdgcn_readfirstlane((unsigned)u), hi = __builtin_amdgcn_readfirstlane((unsigned)(u >> 32));
  return (const char*)(((unsigned long long)hi << 32) | lo);
}
__device__ __forceinline__ void glds(const char* base, unsigned voff, unsigned soff, lchar* l) {
  asm volatile("s_mov_b32 m0, %2\n\ts_nop 0\n\tglobal_load_lds_dwordx4 %0, %1"
               :: "v"(voff), "s"(base + soff), "s"((unsigned)(size_t)l) : "memory");
}

__global__ __launch_bounds__(256) void ln_kernel(const u32x4* __restrict__ x, u32x4* __restrict__ xn, unsigned* __restrict__ flags) {
  __shared__ float red[2][4][8];
  const int wid = threadIdx.x >> 6, lane = threadIdx.x & 63, r = lane & 7, c = lane >> 3;
  const int row = blockIdx.x * 8 + r;
  if (threadIdx.x == 0) flags[blockIdx.x] = 0u;
  u32x4 v[4];
#pragma unroll
  for (int i = 0; i < 4; ++i) v[i] = x[row * 128 + wid * 32 + i * 8 + c];
  float f[32], s = 0.f;
#pragma unroll
  for (int i = 0; i < 4; ++i)
#pragma unroll
    for (int e = 0; e < 4; ++e) { f[i * 8 + 2 * e] = lo16(v[i][e]); f[i * 8 + 2 * e + 1] = hi16(v[i][e]); }
#pragma unroll
  for (int i = 0; i < 32; ++i) s += f[i];
  s += __shfl_xor(s, 8); s += __shfl_xor(s, 16); s += __shfl_xor(s, 32);
  if (lane < 8) red[0][wid][r] = s;
  __syncthreads();
  const float mean = (red[0][0][r] + red[0][1][r] + red[0][2][r] + red[0][3][r]) * (1.f / DM);
  float q = 0.f;
#pragma unroll
  for (int i = 0; i < 32; ++i) { float d = f[i] - mean; q += d * d; }
  q += __shfl_xor(q, 8); q += __shfl_xor(q, 16); q += __shfl_xor(q, 32);
  if (lane < 8) red[1][wid][r] = q;
  __syncthreads();
  const float var = (red[1][0][r] + red[1][1][r] + red[1][2][r] + red[1][3][r]) * (1.f / DM);
  const float m = rbf(mean), sd = rbf(sqrtf(rbf(rbf(var) + 1e-5f)));
#pragma unroll
  for (int i = 0; i < 4; ++i) {
    u32x4 o;
#pragma unroll
    for (int e = 0; e < 4; ++e) o[e] = pk2(rbf(f[i * 8 + 2 * e] - m) / sd, rbf(f[i * 8 + 2 * e + 1] - m) / sd);
    const int ch = wid * 32 + i * 8 + c;
    xn[((row >> 4) * 32 + (ch >> 2)) * 64 + (ch & 3) * 16 + (row & 15)] = o;
  }
}

constexpr int G1_X = 0, G1_H = 65536, G1_W = 69632, G1_WU = 14336, G1_LDS = G1_W + 4 * G1_WU;

template <bool QKV> struct G1 {
  static constexpr int CH = QKV ? 24 : 28, LDW = (QKV ? 3072 : DFF) * 2, NFR = QKV ? 6 : 7, NT = 16;
  static constexpr int VW = QKV ? 6 : 7;
  const char* xn; const char* W; lchar* shm;
  int G, wl, mt; unsigned lane16, xb, hb;
  unsigned wo[4], wb[NFR];
  int n0;
  f32x4 acc[4][NFR], hacc;
  s16x8 xa[4], hx[2];
  s16x4 hl, hh; unsigned wbh;

  __device__ __forceinline__ void issueX(int h, int t, int B) {
#pragma unroll
    for (int a = 0; a < 2; ++a)
#pragma unroll
      for (int b = 0; b < 2; ++b)
        glds(xn, lane16, ((mt * 16 + h * 8 + 2 * wl + a) * 32 + 2 * t + b) * 1024,
             shm + G1_X + (B * 2 + h) * 16384 + ((2 * wl + a) * 2 + b) * 1024);
  }
  __device__ __forceinline__ void issueW(int t, int B) {
    const unsigned so = (t * 64 + G * 32) * LDW;
    lchar* dst = shm + G1_W + (B * 2 + G) * G1_WU + n0 * 1024;
#pragma unroll
    for (int k = 0; k < 3; ++k) glds(W, wo[k], so, dst + k * 1024);
    if (wl < 2) {
      if (QKV) { const int hm = mt > 0 ? mt * 16 - 1 : 0;
        if (G == 0) glds(xn, lane16, (hm * 32 + 2 * t + wl) * 1024, shm + G1_H + B * 2048 + wl * 1024); }
      else glds(W, wo[3], so, dst + 3 * 1024);
    }
  }
  template <int B, int KH, int n> __device__ __forceinline__ void rdW(s16x4* l, s16x4* h) {
    if constexpr (n < NFR) {
      constexpr int o = (B * 2 + KH) * G1_WU + (QKV ? 0 : (n < 6 ? (n >> 1) * 64 : 0));
      constexpr int bi = QKV ? n : (n < 6 ? (n & 1) : 2);
      l[n] = tr_read<o>(wb[bi]); h[n] = tr_read<o + 4 * CH * 16>(wb[bi]);
      rdW<B, KH, n + 1>(l, h);
    } else if constexpr (QKV) { hl = tr_read<(B * 2 + KH) * G1_WU>(wbh); hh = tr_read<(B * 2 + KH) * G1_WU + 4 * CH * 16>(wbh); }
  }
  template <int KH> __device__ __forceinline__ void mma(s16x4* l, s16x4* h, int R) {
    __builtin_amdgcn_s_setprio(1);
#pragma unroll
    for (int n = 0; n < NFR; ++n) {
      s16x8 w = PK8(l[n], h[n]);
#pragma unroll
      for (int mi = 0; mi < 4; ++mi) acc[mi][n] = MFMA(w, xa[mi], acc[mi][n]);
    }
    if (QKV) hacc = MFMA(PK8(hl, hh), hx[KH], hacc);
    __builtin_amdgcn_s_setprio(0);
  }
  template <int B> __device__ __forceinline__ void tile(int t, int R) {
    s16x4 l[NFR], h[NFR];
    xa[0] = rd128<B * 32768 + 0 * 1024>(xb); xa[1] = rd128<B * 32768 + 2 * 1024>(xb);
    xa[2] = rd128<B * 32768 + 4 * 1024>(xb); xa[3] = rd128<B * 32768 + 6 * 1024>(xb);
    if (QKV) { hx[0] = rd128<B * 2048>(hb); hx[1] = rd128<B * 2048 + 1024>(hb); }
    rdW<B, 0, 0>(l, h);
    if (t + 1 < NT) {
      if (G == 0) issueX(1, t + 1, B ^ 1); else issueW(t + 1, B ^ 1);
      WAIT_V(7);
    } else WAIT_V(0);
    WAIT_L0(); BAR(); SCHED();
    mma<0>(l, h, R);
    SCHED(); BAR(); SCHED();
    xa[0] = rd128<B * 32768 + 1 * 1024>(xb); xa[1] = rd128<B * 32768 + 3 * 1024>(xb);
    xa[2] = rd128<B * 32768 + 5 * 1024>(xb); xa[3] = rd128<B * 32768 + 7 * 1024>(xb);
    rdW<B, 1, 0>(l, h);
    if (t + 2 < NT) {
      if (G == 0) issueW(t + 2, B); else issueX(0, t + 2, B);
      WAIT_V(7);
    } else WAIT_V(3);
    WAIT_L0(); BAR(); SCHED();
    mma<1>(l, h, R);
    SCHED(); BAR(); SCHED();
  }

  __device__ __forceinline__ void run(const char* xn_, const char* Wg, char* acat, const char* pf, int mt_, int j, lchar* shm_) {
    ST(0, 0); ST(0, 1);
    xn = xn_; shm = shm_; mt = mt_; W = Wg;
    const int cb0 = QKV ? j * 128 : j * 448;
    const int wid = __builtin_amdgcn_readfirstlane(threadIdx.x >> 6), lane = threadIdx.x & 63;
    G = wid >> 2; wl = wid & 3;
    const int wr = wl >> 1, wc = wl & 1, R = G * 2 + wr;
    const int g = lane >> 4, q = (lane >> 2) & 3, p = lane & 3;
    const unsigned l0 = (unsigned)(size_t)shm;
    lane16 = lane * 16;
    n0 = QKV ? 3 * wl : (wl < 2 ? 4 * wl : 2 + 3 * wl);
    if (G == 1) issueX(0, 0, 0);
#pragma unroll
    for (int k = 0; k < 4; ++k) {
      int n = n0 + k; n = n > (CH / 2 - 1) ? (CH / 2 - 1) : n;
      const int i = n * 64 + lane, r = i / CH, pos = i % CH;
      const int ch = QKV ? (pos ^ ((((r >> 1) & 1) << 2) | ((r >> 3) & 1))) : (pos ^ ((r >> 3) & 1));
      int cb;
      if (QKV) { const int F = ch >> 2, hd = F / 3, part = F % 3; cb = part * 2048 + hd * 64 + (ch & 3) * 16; }
      else cb = ch * 16;
      wo[k] = r * LDW + cb + cb0;
    }
    issueW(0, 0);
    if (G == 0) { issueX(1, 0, 0); issueW(1, 1); } else issueX(0, 1, 1);
    SCHED();
    xb = l0 + G1_X + G * 16384 + wr * 8192 + lane16;
    hb = l0 + G1_H + lane16;
    {
      const unsigned lb = l0 + G1_W + ((8 * g + q) * CH + (p ^ (g & 1))) * 16;
      if (QKV) {
#pragma unroll
        for (int n = 0; n < 6; ++n) wb[n] = lb + (((3 * wc + (n >> 1)) ^ (q >> 1)) * 64) + (((p & 1) ^ (n & 1)) * 8);
        wbh = lb + (((3 * wc + 1 + (R >> 1)) ^ (q >> 1)) * 64) + (((p & 1) ^ (R & 1)) * 8);
      } else {
        wb[0] = lb + wc * 256 + (p & 1) * 8; wb[1] = lb + wc * 256 + ((p & 1) ^ 1) * 8;
        wb[2] = lb + 192 + ((p & 1) ^ wc) * 8;
      }
    }
#pragma unroll
    for (int mi = 0; mi < 4; ++mi)
#pragma unroll
      for (int n = 0; n < NFR; ++n) { acc[mi][n] = (f32x4){0.f, 0.f, 0.f, 0.f}; asm volatile("" : "+v"(acc[mi][n])); }
    hacc = (f32x4){0.f, 0.f, 0.f, 0.f};
    SCHED();
    WAIT_V(7);
    BAR();
    ST(0, 2);
    if (G == 1) BAR();
    SCHED();
    unsigned pfd = 0;
    for (int t = 0; t < NT; t += 2) {
      if (t >= 2 && t < 14 && lane < 6) asm volatile("global_load_dword %0, %1, off" : "+v"(pfd) : "v"(pf + (wid * 36 + (t - 2) * 3 + lane) * 128) : "memory");
      tile<0>(t, R); tile<1>(t + 1, R);
    }
    if (G == 0) BAR();
    asm volatile("" :: "v"(pfd));
    SCHED();
    ST(0, 3);
    const int mb0 = mt * 16 + R * 4;
    if (!QKV) {
      char* dst = acat + (size_t)((mb0 * KB2 + 32 + 7 * j) * 1024);
#pragma unroll
      for (int mi = 0; mi < 4; ++mi) {
#pragma unroll
        for (int u = 0; u < 3; ++u) {
          const f32x4 a = acc[mi][2 * u], b = acc[mi][2 * u + 1];
          u32x4 o = {relu2(pk2(a[0], a[1])), relu2(pk2(a[2], a[3])), relu2(pk2(b[0], b[1])), relu2(pk2(b[2], b[3]))};
          *(u32x4*)(dst + (mi * KB2 + 4 * wc + u) * 1024 + lane16) = o;
        }
        const f32x4 a = acc[mi][6];
        u32x2 o = {relu2(pk2(a[0], a[1])), relu2(pk2(a[2], a[3]))};
        *(u32x2*)(dst + (mi * KB2 + 3) * 1024 + wc * 8 + lane16) = o;
      }
    } else {
      u32x4 fr[4][3];
#pragma unroll
      for (int mi = 0; mi < 4; ++mi)
#pragma unroll
        for (int u = 0; u < 3; ++u) {
          const f32x4 a = acc[mi][2 * u], b = acc[mi][2 * u + 1];
          fr[mi][u] = (u32x4){pk2(a[0], a[1]), pk2(a[2], a[3]), pk2(b[0], b[1]), pk2(b[2], b[3])};
        }
      lchar* vimg = shm + wc * 17408;
      lchar* kblk = shm + 34816 + wc * 4096;
      const int rr = lane & 15;
#pragma unroll
      for (int mi = 0; mi < 4; ++mi)
        *(__attribute__((address_space(3))) u32x4*)(vimg + (16 + R * 64 + mi * 16 + rr) * 64 + g * 16) = fr[mi][2];
      if (R < 3) *(__attribute__((address_space(3))) u32x4*)(kblk + (R + 1) * 1024 + lane16) = fr[3][1];
      {
        u32x2 hv = {pk2(hacc[0], hacc[1]), pk2(hacc[2], hacc[3])};
        lchar* d = ((R < 2) ? (kblk + lane16) : (vimg + rr * 64 + g * 16)) + (R & 1) * 8;
        *(__attribute__((address_space(3))) u32x2*)d = hv;
      }
      const f32x4 z = {0.f, 0.f, 0.f, 0.f};
      f32x4 s0[4], s1[4];
#pragma unroll
      for (int mi = 0; mi < 4; ++mi) {
        s1[mi] = MFMA(__builtin_bit_cast(s16x8, fr[mi][1]), __builtin_bit_cast(s16x8, fr[mi][0]), z);
        if (mi) s0[mi] = MFMA(__builtin_bit_cast(s16x8, fr[mi - 1][1]), __builtin_bit_cast(s16x8, fr[mi][0]), z);
      }
      __syncthreads();
      const u32x4 kprev = *(__attribute__((address_space(3))) u32x4*)(kblk + R * 1024 + lane16);
      s0[0] = MFMA(__builtin_bit_cast(s16x8, kprev), __builtin_bit_cast(s16x8, fr[0][0]), z);
      const unsigned vb = l0 + wc * 17408 + (R * 64 + 4 * g + q) * 64 + p * 16;
      s16x4 va[4], vc[4], vd[4], ve[4];
      va[0] = tr_read<0>(vb); vc[0] = tr_read<8>(vb); vd[0] = tr_read<1024>(vb); ve[0] = tr_read<1032>(vb);
      va[1] = tr_read<1024>(vb); vc[1] = tr_read<1032>(vb); vd[1] = tr_read<2048>(vb); ve[1] = tr_read<2056>(vb);
      va[2] = tr_read<2048>(vb); vc[2] = tr_read<2056>(vb); vd[2] = tr_read<3072>(vb); ve[2] = tr_read<3080>(vb);
      va[3] = tr_read<3072>(vb); vc[3] = tr_read<3080>(vb); vd[3] = tr_read<4096>(vb); ve[3] = tr_read<4104>(vb);
      const bool par = rr & 1;
      const int k0 = 4 * g + (rr & 1);
      const bool c0 = k0 <= rr, c1 = k0 + 2 <= rr;
      const float scale = 0.17677669529663687f;
      float e0[4], e1[4], mx[4], sm[4];
#pragma unroll
      for (int mi = 0; mi < 4; ++mi) {
        const float a0 = c0 ? (par ? s1[mi][1] : s1[mi][0]) : (par ? s0[mi][1] : s0[mi][0]);
        const float a1 = c1 ? (par ? s1[mi][3] : s1[mi][2]) : (par ? s0[mi][3] : s0[mi][2]);
        e0[mi] = rbf(rbf(a0) * scale); e1[mi] = rbf(rbf(a1) * scale);
        if (mi == 0 && mb0 == 0) { e0[0] = c0 ? e0[0] : -1e9f; e1[0] = c1 ? e1[0] : -1e9f; }
        mx[mi] = fmaxf(e0[mi], e1[mi]);
      }
#pragma unroll
      for (int mi = 0; mi < 4; ++mi) mx[mi] = red16<true>(mx[mi]);
#pragma unroll
      for (int mi = 0; mi < 4; ++mi) mx[mi] = red32<true>(mx[mi]);
#pragma unroll
      for (int mi = 0; mi < 4; ++mi) { e0[mi] = __expf(e0[mi] - mx[mi]); e1[mi] = __expf(e1[mi] - mx[mi]); sm[mi] = e0[mi] + e1[mi]; }
#pragma unroll
      for (int mi = 0; mi < 4; ++mi) sm[mi] = red16<false>(sm[mi]);
#pragma unroll
      for (int mi = 0; mi < 4; ++mi) sm[mi] = red32<false>(sm[mi]);
      WAIT_L0(); SCHED();
      char* dst = acat + (size_t)((mb0 * KB2 + 2 * j + wc) * 1024) + lane16;
#pragma unroll
      for (int mi = 0; mi < 4; ++mi) {
        const float inv = __builtin_amdgcn_rcpf(sm[mi]);
        const unsigned pr = pk2(e0[mi] * inv, e1[mi] * inv);
        const unsigned p0 = par ? (pr << 16) : (pr & 0xffffu), p1 = par ? (pr & 0xffff0000u) : (pr >> 16);
        const u32x4 pp = {c0 ? 0u : p0, c1 ? 0u : p1, c0 ? p0 : 0u, c1 ? p1 : 0u};
        const s16x8 pf8 = __builtin_bit_cast(s16x8, pp);
        const f32x4 o0 = MFMA(PK8(va[mi], vd[mi]), pf8, z);
        const f32x4 o1 = MFMA(PK8(vc[mi], ve[mi]), pf8, z);
        const u32x4 o = {pk2(o0[0], o0[1]), pk2(o0[2], o0[3]), pk2(o1[0], o1[1]), pk2(o1[2], o1[3])};
        *(u32x4*)(dst + mi * KB2 * 1024) = o;
      }
    }
    ST(0, 4);
    ST(0, 5); ST(0, 6); ST(0, 8); ST(0, 7);
  }
};

__global__ __launch_bounds__(512, 2) void gemm1_kernel(const char* __restrict__ xn, const char* __restrict__ wqkv,
                                                       const char* __restrict__ w1, char* __restrict__ acat,
                                                       const char* __restrict__ wo, const char* __restrict__ w2) {
  extern __shared__ __attribute__((aligned(16))) char shm[];
  const int xcd = blockIdx.x & 7, idx = blockIdx.x >> 3;
  const int mt = (xcd & 1) * 4 + (idx & 3), ni = idx >> 2, j = (xcd >> 1) * 4 + (ni & 3);
  const int b = blockIdx.x;
  const char* pf = b < 57 ? wo + (size_t)(b < 56 ? b * 18 : 1006) * 2048 : w2 + (size_t)(b < 255 ? (b - 57) * 18 + 2 : 3566) * 2048;
  if (ni < 4) { G1<false> k; k.run(xn, w1, acat, pf, mt, j, (lchar*)shm); }
  else { G1<true> k; k.run(xn, wqkv, acat, pf, mt, j, (lchar*)shm); }
}

#define G2NST 5
constexpr int G2_NST = G2NST, G2_STG = 32768, G2_LDS = G2_NST * G2_STG, G2_NK = 36, G2_PFD = G2_NST + 8;
typedef __attribute__((address_space(3))) f32x4 lf4;
typedef __attribute__((address_space(1))) unsigned gu32;

__global__ __launch_bounds__(768, 3) void gemm2_kernel(const char* __restrict__ acat, const char* __restrict__ wo_,
                                                       const char* __restrict__ w2_, const char* __restrict__ x,
                                                       char* __restrict__ out, char* __restrict__ exch,
                                                       unsigned* __restrict__ flags) {
  extern __shared__ __attribute__((aligned(16))) char shm_[];
  lchar* shm = (lchar*)shm_;
  ST(1, 0); ST(1, 1);
  const int xcd = blockIdx.x & 7, idx = blockIdx.x >> 3;
  const int ks = xcd & 1, mt = (xcd >> 1) * 4 + (idx & 3), nt = idx >> 2;
  const int wid = __builtin_amdgcn_readfirstlane(threadIdx.x >> 6), lane = threadIdx.x & 63;
  const int wl = wid & 3, G = wid >> 2;
  const unsigned lane16 = lane * 16;
  if (G == 2) {
    const char* A = uni(acat); const char* Wo = uni(wo_); const char* W2 = uni(w2_);
    const unsigned a0 = ((mt * 8 + 2 * wl) * KB2 + ks * 2 * G2_NK) * 1024;
    unsigned wo[4];
#pragma unroll
    for (int k = 0; k < 4; ++k) {
      const int r = wl * 16 + k * 4 + (lane >> 4), pos = lane & 15;
      wo[k] = (r ^ (((r >> 3) & 1) << 2)) * 2048 + (pos ^ (((r >> 3) & 1) | ((r & 3) << 2))) * 16 + nt * 256;
    }
    auto issue = [&](int s, int buf) {
      const int kt = ks * G2_NK + s;
      lchar* dst = shm + buf * G2_STG + wl * 4096;
      const char* Wr = kt < 16 ? Wo : W2;
      const unsigned so = (kt < 16 ? kt : kt - 16) * 131072;
#pragma unroll
      for (int k = 0; k < 4; ++k) {
        glds(A, lane16, a0 + s * 2048 + (k >> 1) * (KB2 * 1024) + (k & 1) * 1024, dst + k * 1024);
        glds(Wr, wo[k], so, dst + 16384 + k * 1024);
      }
    };
    issue(0, 0); issue(1, 1); issue(2, 2);
    WAIT_V(16); BAR();
    issue(3, 3);
    WAIT_V(16); BAR();
    int ib = 4;
    for (int j = 0; j < G2_NK; ++j) {
      if (j + 4 < G2_NK) { issue(j + 4, ib); WAIT_V(16); }
      else if (j + 3 < G2_NK) WAIT_V(8); else WAIT_V(0);
      BAR();
      ib = ib + 1 == G2_NST ? 0 : ib + 1;
    }
    BAR(); BAR(); BAR();
    return;
  }
  const int wr = wl >> 1, wc = wl & 1;
  const int g = lane >> 4, q = (lane >> 2) & 3, p = lane & 3;
  const unsigned l0 = (unsigned)(size_t)shm;
  unsigned ab[4], wb[4];
#pragma unroll
  for (int mi = 0; mi < 4; ++mi) ab[mi] = l0 + wr * 8192 + (mi ^ (2 * G + ks)) * 2048 + lane16;
#pragma unroll
  for (int n = 0; n < 4; ++n)
    wb[n] = l0 + 16384 + ((8 * g + q) * 16 + 4 * ((wc * 2 + (n >> 1)) ^ q) + (p ^ (g & 1))) * 16 + (((p & 1) ^ (n & 1)) * 8);
  f32x4 acc[4][4];
#pragma unroll
  for (int mi = 0; mi < 4; ++mi)
#pragma unroll
    for (int n = 0; n < 4; ++n) { acc[mi][n] = (f32x4){0.f, 0.f, 0.f, 0.f}; asm volatile("" : "+v"(acc[mi][n])); }
  SCHED();
  BAR();
  ST(1, 2);
#define PH(a_, b_)
  if (G == 1) BAR();
  SCHED();
  unsigned bo = G * G2_STG;
  for (int s = G; s < G2_NK; s += 2) {
    s16x8 a[4][2]; s16x4 l[2][4], h[2][4];
    unsigned pa[4], pw[4];
#pragma unroll
    for (int i = 0; i < 4; ++i) { pa[i] = ab[i] + bo; pw[i] = wb[i] + bo; }
#pragma unroll
    for (int i = 0; i < 4; ++i) { a[i][0] = rd128<0>(pa[i]); l[0][i] = tr_read<0>(pw[i]); h[0][i] = tr_read<1024>(pw[i]); }
#pragma unroll
    for (int i = 0; i < 4; ++i) { a[i][1] = rd128<1024>(pa[i]); l[1][i] = tr_read<8192>(pw[i]); h[1][i] = tr_read<9216>(pw[i]); }
    WAIT_L(12); BAR(); SCHED();
    __builtin_amdgcn_s_setprio(1);
#pragma unroll
    for (int kh = 0; kh < 2; ++kh) {
      if (kh) { SCHED(); WAIT_L0(); SCHED(); }
#pragma unroll
      for (int n = 0; n < 4; ++n) {
        const s16x8 w = PK8(l[kh][n], h[kh][n]);
#pragma unroll
        for (int mi = 0; mi < 4; ++mi) acc[mi][n] = MFMA(w, a[mi][kh], acc[mi][n]);
      }
    }
    __builtin_amdgcn_s_setprio(0);
    SCHED(); BAR(); SCHED();
    bo += 2 * G2_STG; if (bo >= G2_NST * G2_STG) bo -= G2_NST * G2_STG;
  }
  if (G == 0) BAR();
  SCHED();
  ST(1, 3);
  const int row = mt * 128 + wr * 64 + (2 * G + ks) * 16 + (lane & 15);
  const size_t ooff = (size_t)row * 2048 + (nt * 128 + wc * 64 + g * 8) * 2;
  const u32x4 xv0 = *(const u32x4*)(x + ooff), xv1 = *(const u32x4*)(x + ooff + 64);
#pragma unroll
  for (int i = 0; i < 2; ++i)
#pragma unroll
    for (int n = 0; n < 4; ++n) *(lf4*)(shm + ((wid * 2 + i) * 4 + n) * 1024 + lane16) = acc[2 + i][n];
  WAIT_L0(); BAR(); SCHED();
  f32x4 keep[4], send[4];
#pragma unroll
  for (int n = 0; n < 4; ++n) {
    send[n] = acc[1][n] + *(lf4*)(shm + (((wid ^ 4) * 2 + 1) * 4 + n) * 1024 + lane16);
    keep[n] = acc[0][n] + *(lf4*)(shm + (((wid ^ 4) * 2 + 0) * 4 + n) * 1024 + lane16);
  }
  const int tile = mt * 8 + nt;
  const rsrc_t rs = mkrs(exch + (size_t)tile * 65536, 65536);
#pragma unroll
  for (int n = 0; n < 4; ++n)
    __builtin_amdgcn_raw_buffer_store_b128(__builtin_bit_cast(u32x4, send[n]), rs, ks * 32768 + (wid * 4 + n) * 1024 + lane16, 0, 16);
  ST(1, 4);
  WAIT_V(0); BAR();
  ST(1, 5);
  if (threadIdx.x == 0) {
    __hip_atomic_store((gu32*)(flags + tile * 2 + ks), 1u, __ATOMIC_RELAXED, __HIP_MEMORY_SCOPE_AGENT);
    while (__hip_atomic_load((gu32*)(flags + tile * 2 + (ks ^ 1)), __ATOMIC_RELAXED, __HIP_MEMORY_SCOPE_AGENT) == 0u) __builtin_amdgcn_s_sleep(1);
  }
  asm volatile("" ::: "memory");
  BAR();
  ST(1, 6);
  asm volatile("" ::: "memory");
#pragma unroll
  for (int n = 0; n < 4; ++n)
    keep[n] += __builtin_bit_cast(f32x4, __builtin_amdgcn_raw_buffer_load_b128(rs, (ks ^ 1) * 32768 + (wid * 4 + n) * 1024 + lane16, 0, 16));
  const bool odd = g & 1;
#pragma unroll
  for (int u = 0; u < 2; ++u) {
    f32x4 lo, hi;
#pragma unroll
    for (int e = 0; e < 4; ++e) { lo[e] = odd ? keep[2 * u + 1][e] : keep[2 * u][e]; hi[e] = odd ? keep[2 * u][e] : keep[2 * u + 1][e]; }
    const u32x4 xv = u ? xv1 : xv0;
    const u32x4 o = {pk2(lo[0] + lo16(xv[0]), lo[1] + hi16(xv[0])), pk2(lo[2] + lo16(xv[1]), lo[3] + hi16(xv[1])),
                     pk2(hi[0] + lo16(xv[2]), hi[1] + hi16(xv[2])), pk2(hi[2] + lo16(xv[3]), hi[3] + hi16(xv[3]))};
    *(u32x4*)(out + ooff + u * 64) = o;
  }
  ST(1, 8); ST(1, 7);
}

extern "C" void kernel_launch(void* const* d_in, const int* in_sizes, int n_in, void* d_out, int out_size,
                              void* d_ws, size_t ws_size, hipStream_t stream) {
  const char* x = (const char*)d_in[0];
  const char* wqkv = (const char*)d_in[1];
  const char* wo = (const char*)d_in[2];
  const char* w1 = (const char*)d_in[4];
  const char* w2 = (const char*)d_in[5];
  char* ws = (char*)d_ws;
  (void)hipFuncSetAttribute((const void*)gemm1_kernel, hipFuncAttributeMaxDynamicSharedMemorySize, G1_LDS);
  (void)hipFuncSetAttribute((const void*)gemm2_kernel, hipFuncAttributeMaxDynamicSharedMemorySize, G2_LDS);
  ln_kernel<<<256, 256, 0, stream>>>((const u32x4*)x, (u32x4*)(ws + WS_XN), (unsigned*)(ws + WS_FLAG));
  gemm1_kernel<<<256, 512, G1_LDS, stream>>>(ws + WS_XN, wqkv, w1, ws + WS_ACAT, wo, w2);
  gemm2_kernel<<<256, 768, G2_LDS, stream>>>(ws + WS_ACAT, wo, w2, x, (char*)d_out, ws + WS_EXCH, (unsigned*)(ws + WS_FLAG));
}
```

```cpp
#include <hip/hip_runtime.h>
#include <hip/hip_bf16.h>

typedef short s16x4 __attribute__((ext_vector_type(4)));
typedef short s16x8 __attribute__((ext_vector_type(8)));
typedef __bf16 bf16x2 __attribute__((ext_vector_type(2)));
typedef float f32x4 __attribute__((ext_vector_type(4)));
typedef unsigned u32x4 __attribute__((ext_vector_type(4)));
typedef unsigned u32x2 __attribute__((ext_vector_type(2)));
typedef __attribute__((address_space(3))) char lchar;
typedef __attribute__((address_space(3))) unsigned lu32;

template <int V> struct IC { static constexpr int value = V; };
#define BAR() __builtin_amdgcn_s_barrier()
#define SCHED() __builtin_amdgcn_sched_barrier(0)
#define WAIT_V(n) asm volatile("s_waitcnt vmcnt(%0)" ::"n"(n) : "memory")
#define WAIT_L(n) asm volatile("s_waitcnt lgkmcnt(%0)" ::"n"(n) : "memory")
#define WAIT_L0() asm volatile("s_waitcnt lgkmcnt(0)" ::: "memory")
#define PK8(L, H) (s16x8){L[0], L[1], L[2], L[3], H[0], H[1], H[2], H[3]}
#define MFMA(a, b, c) __builtin_amdgcn_mfma_f32_16x16x32_bf16(a, b, c, 0, 0, 0)

#define ST(k, i)
constexpr int SEQ = 2048, DM = 1024, DFF = 3584, KC = 4608, KB2 = KC / 32;
constexpr size_t WS_XN = 0, WS_ACAT = 4u << 20, WS_EXCH = 24u << 20, WS_FLAG = 40u << 20;

__device__ __forceinline__ unsigned pk2(float a, float b) {
  bf16x2 v = {(__bf16)a, (__bf16)b};
  return __builtin_bit_cast(unsigned, v);
}
__device__ __forceinline__ float rbf(float a) { return (float)(__bf16)a; }
__device__ __forceinline__ float lo16(unsigned u) { return __builtin_bit_cast(float, u << 16); }
__device__ __forceinline__ float hi16(unsigned u) { return __builtin_bit_cast(float, u & 0xffff0000u); }

typedef short s16x2 __attribute__((ext_vector_type(2)));
__device__ __forceinline__ unsigned relu2(unsigned u) {
  const s16x2 z = {0, 0};
  return __builtin_bit_cast(unsigned, __builtin_elementwise_max(__builtin_bit_cast(s16x2, u), z));
}
template <bool MAX> __device__ __forceinline__ float red16(float v) {
  const unsigned u = __builtin_bit_cast(unsigned, v);
  auto r = __builtin_amdgcn_permlane16_swap(u, u, false, false);
  const unsigned r0 = r[0], r1 = r[1];
  const float a = __builtin_bit_cast(float, r0), b = __builtin_bit_cast(float, r1);
  return MAX ? fmaxf(a, b) : a + b;
}
template <bool MAX> __device__ __forceinline__ float red32(float v) {
  const unsigned u = __builtin_bit_cast(unsigned, v);
  auto r = __builtin_amdgcn_permlane32_swap(u, u, false, false);
  const unsigned r0 = r[0], r1 = r[1];
  const float a = __builtin_bit_cast(float, r0), b = __builtin_bit_cast(float, r1);
  return MAX ? fmaxf(a, b) : a + b;
}
template <int OFF> __device__ __forceinline__ s16x4 tr_read(unsigned a) {
  s16x4 r; asm volatile("ds_read_b64_tr_b16 %0, %1 offset:%2" : "=&v"(r) : "v"(a), "i"(OFF) : "memory"); return r;
}
template <int OFF> __device__ __forceinline__ s16x8 rd128(unsigned a) {
  s16x8 r; asm volatile("ds_read_b128 %0, %1 offset:%2" : "=&v"(r) : "v"(a), "i"(OFF) : "memory"); return r;
}
typedef __amdgpu_buffer_rsrc_t rsrc_t;
__device__ __forceinline__ rsrc_t mkrs(const void* p, unsigned bytes) {
  return __builtin_amdgcn_make_buffer_rsrc((void*)p, 0, bytes, 0x00020000);
}
__device__ __forceinline__ const char* uni(const char* p) {
  const unsigned long long u = (unsigned long long)p;
  const unsigned lo = __builtin_amdgcn_readfirstlane((unsigned)u), hi = __builtin_amdgcn_readfirstlane((unsigned)(u >> 32));
  return (const char*)(((unsigned long long)hi << 32) | lo);
}
#define NX_ 4
#define NQ_ 3
#define NW_ 4
#define PFL 15
#define PFR 13
#define STPOL ""
__device__ __forceinline__ void st128(char* p, u32x4 v) { asm volatile("global_store_dwordx4 %0, %1, off " STPOL "\n\ts_nop 1" :: "v"(p), "v"(v) : "memory"); }
__device__ __forceinline__ void st64(char* p, u32x2 v) { asm volatile("global_store_dwordx2 %0, %1, off " STPOL "\n\ts_nop 1" :: "v"(p), "v"(v) : "memory"); }
__device__ __forceinline__ void glds(const char* base, unsigned voff, unsigned soff, lchar* l) {
  asm volatile("s_mov_b32 m0, %2\n\ts_nop 0\n\tglobal_load_lds_dwordx4 %0, %1"
               :: "v"(voff), "s"(base + soff), "s"((unsigned)(size_t)l) : "memory");
}

constexpr int G1_XU = 34816, G1_WH = 3 * G1_XU, G1_WQ = G1_WH + 32768, G1_ONE = G1_WQ + 24576, G1_LDS = G1_ONE + 16, G1_NT = 16;
typedef __attribute__((address_space(3))) u32x4 lu4;
typedef __attribute__((address_space(3))) u32x2 lu2;
typedef __attribute__((address_space(3))) f32x4 lf4;
typedef float f32x2 __attribute__((ext_vector_type(2)));
typedef __attribute__((address_space(3))) f32x2 lf2;
typedef __attribute__((address_space(3))) float lf1;

#define MM "v_mfma_f32_16x16x32_bf16 "
#define AMMA(c, a, b) asm volatile(MM "%0, %1, %2, %0" : "+v"(c) : "v"(a), "v"(b))
#define MMA_DONE() asm volatile("s_nop 15\n\ts_nop 15" ::: "memory")
__device__ __forceinline__ float sel(unsigned m, float a, float b) {
  return __builtin_bit_cast(float, (__builtin_bit_cast(unsigned, a) & m) | (__builtin_bit_cast(unsigned, b) & ~m));
}
__device__ __forceinline__ float redg(float v) { return red32<false>(red16<false>(v)); }
__device__ __forceinline__ f32x2 ln_stat(float s, float q) {
  const float mean = s * (1.f / DM), var = fmaxf(q * (1.f / DM) - mean * mean, 0.f);
  return (f32x2){rbf(mean), 1.f / rbf(sqrtf(rbf(rbf(var) + 1e-5f)))};
}
__device__ __forceinline__ f32x4 nrm(f32x4 a, f32x4 cs, f32x2 st) { return (a - st[0] * cs) * st[1]; }

__global__ __launch_bounds__(512, 2) void gemm1_kernel(const char* __restrict__ x_, const char* __restrict__ wqkv_,
                                                       const char* __restrict__ w1_, char* __restrict__ acat,
                                                       const char* __restrict__ wo_, const char* __restrict__ w2_,
                                                       unsigned* __restrict__ flags) {
  extern __shared__ __attribute__((aligned(16))) char shm_[];
  lchar* shm = (lchar*)shm_;
  ST(0, 0); ST(0, 1);
  const int xcd = blockIdx.x & 7, idx = blockIdx.x >> 3;
  const int mt = (xcd & 1) * 4 + (idx & 3), j = (xcd >> 1) * 8 + (idx >> 2);
  const int wid = __builtin_amdgcn_readfirstlane(threadIdx.x >> 6), lane = threadIdx.x & 63;
  const int G = wid >> 2, R = wid & 3;
  const int g = lane >> 4, q = (lane >> 2) & 3, p = lane & 3, rr = lane & 15;
  const unsigned l0 = (unsigned)(size_t)shm, lane16 = lane * 16;
  const char* x = uni(x_);
#define ROT 0
  const int rot = ROT;
  auto tk = [&](int t) { return (t + rot) & (G1_NT - 1); };
  const unsigned xo0 = (lane >> 3) * 2048 + ((lane & 7) ^ g) * 16, xo1 = (lane >> 3) * 2048 + ((lane & 7) ^ (4 + g)) * 16;
  auto issueX = [&](int h, int t, int xbuf) {
#pragma unroll
    for (int a = 0; a < NX_; ++a)
      glds(x, (a & 1) ? xo1 : xo0, (mt * 256 + h * 128 + R * 32 + a * 8) * 2048 + tk(t) * 128, shm + xbuf + (h * 16 + R * 4 + a) * 1024);
  };
  const unsigned xr0 = l0 + (R * 64 + rr) * 128 + (g ^ (rr >> 1)) * 16, xr1 = xr0 ^ 64;
  f32x4 acc[4][7];
  const f32x4 z = {0.f, 0.f, 0.f, 0.f};
  f32x4 gs[2] = {z, z}, gq[2] = {z, z}, cs[2] = {z, z};
  s16x8 ones = {0x3f80, 0x3f80, 0x3f80, 0x3f80, 0x3f80, 0x3f80, 0x3f80, 0x3f80};
  asm volatile("" : "+v"(ones), "+v"(gs[0]), "+v"(gs[1]), "+v"(gq[0]), "+v"(gq[1]), "+v"(cs[0]), "+v"(cs[1]));
  lf2* stat = (lf2*)shm;
  lf1* csl = (lf1*)(shm + 2304);
  lf1* hst = (lf1*)(shm + 3584);
  auto diag = [&](f32x4 d) { const unsigned m1 = 0u - (rr & 1), m2 = 0u - ((rr >> 1) & 1); return sel(m2, sel(m1, d[3], d[2]), sel(m1, d[1], d[0])); };
  auto put_stats = [&]() {
    if (g == q) { stat[R * 64 + G * 32 + rr] = ln_stat(gs[0][0], diag(gq[0])); stat[R * 64 + G * 32 + 16 + rr] = ln_stat(gs[1][0], diag(gq[1])); }
  };
  const int fo = j & 1;
  const int mb0 = mt * 16 + R * 4;
  unsigned pfd = 0;

  if (G == 0) {
    const char* wq = uni(wqkv_);
    unsigned wo[3];
#pragma unroll
    for (int k = 0; k < 3; ++k) {
      const int i = (3 * R + k) * 64 + lane, r = i / 12, ch = (i % 12) ^ ((r >> 3) & 1);
      wo[k] = r * 6144 + (ch >> 2) * 2048 + j * 64 + (ch & 3) * 16;
    }
    const int hrow = mt > 0 ? mt * 256 - 16 : 0;
    auto issueQ = [&](int t, int B) {
#pragma unroll
      for (int k = 0; k < NQ_; ++k) glds(wq, wo[k], tk(t) * 64 * 6144, shm + G1_WQ + B * 12288 + (3 * R + k) * 1024);
    };
    auto issueH = [&](int t, int xbuf) {
      if (R < 2) glds(x, R ? xo1 : xo0, (hrow + R * 8) * 2048 + tk(t) * 128, shm + xbuf + (32 + R) * 1024);
    };
    issueX(1, 0, 0); issueQ(0, 0); issueH(0, 0); issueH(1, G1_XU); issueX(1, 1, G1_XU);
    SCHED();
    if (threadIdx.x == 0) flags[blockIdx.x] = 0u;
    unsigned wb[7];
#pragma unroll
    for (int n = 0; n < 7; ++n) {
      const int F = n < 6 ? ((n >> 1) + R) % 3 + fo : (fo ? 0 : 3), f = n < 6 ? (n & 1) : fo;
      wb[n] = l0 + G1_WH + ((8 * g + q) * 16 + 4 * (F ^ q) + (p ^ (g & 1))) * 16 + (((p & 1) ^ f) * 8);
    }
#pragma unroll
    for (int mi = 0; mi < 4; ++mi)
#pragma unroll
      for (int n = 0; n < 7; ++n) { acc[mi][n] = (f32x4){0.f, 0.f, 0.f, 0.f}; asm volatile("" : "+v"(acc[mi][n])); }
    SCHED();
    WAIT_V(7); BAR();
    ST(0, 2);
    SCHED();
    int xc = 0, xn2 = 2 * G1_XU;
    auto half = [&](auto Bc, auto Kc, int t) {
      constexpr int B = decltype(Bc)::value, KH = decltype(Kc)::value;
      s16x8 xa[4]; s16x4 l[7], h[7];
      const unsigned xp = (KH ? xr1 : xr0) + xc;
      xa[0] = rd128<0>(xp); xa[1] = rd128<2048>(xp); xa[2] = rd128<4096>(xp); xa[3] = rd128<6144>(xp);
#pragma unroll
      for (int n = 0; n < 7; ++n) { l[n] = tr_read<B * 16384 + KH * 8192>(wb[n]); h[n] = tr_read<B * 16384 + KH * 8192 + 1024>(wb[n]); }
      if (KH == 0) {
        if (t + 1 < G1_NT) { issueQ(t + 1, B ^ 1); if (t + 2 < G1_NT) issueH(t + 2, xn2); WAIT_V(7); } else WAIT_V(0);
      } else {
        if (t + 2 < G1_NT) { issueX(1, t + 2, xn2); WAIT_V(7); } else WAIT_V(3);
      }
      WAIT_L0(); BAR(); SCHED();
      __builtin_amdgcn_s_setprio(1);
      asm volatile("s_cmp_eq_u32 %6, 3\n\ts_cbranch_scc1 1f\n\t" MM "%0, %2, %5, %0\n\t" MM "%1, %3, %5, %1\n\ts_branch 2f\n"
                   "1:\n\t" MM "%0, %4, %5, %0\n2:"
                   : "+v"(cs[0]), "+v"(cs[1]) : "v"(PK8(l[0], h[0])), "v"(PK8(l[1], h[1])), "v"(PK8(l[6], h[6])), "v"(ones), "s"(R) : "scc");
      SCHED();
#pragma unroll
      for (int n = 0; n < 7; ++n) {
        const s16x8 w = PK8(l[n], h[n]);
#pragma unroll
        for (int mi = 0; mi < 4; ++mi) AMMA(acc[mi][n], w, xa[mi]);
      }
#pragma unroll
      for (int i = 0; i < 2; ++i) { AMMA(gs[i], ones, xa[i]); AMMA(gq[i], xa[i], xa[i]); }
      __builtin_amdgcn_s_setprio(0);
      SCHED(); BAR(); SCHED();
    };
    for (int t = 0; t < G1_NT; t += 2) {
      half(IC<0>{}, IC<0>{}, t); half(IC<0>{}, IC<1>{}, t);
      xn2 = xc; xc = xc == 2 * G1_XU ? 0 : xc + G1_XU;
      half(IC<1>{}, IC<0>{}, t + 1); half(IC<1>{}, IC<1>{}, t + 1);
      xn2 = xc; xc = xc == 2 * G1_XU ? 0 : xc + G1_XU;
    }
    BAR(); MMA_DONE(); SCHED();
    ST(0, 3);
    put_stats();
    if (rr == 0) { *(lf4*)(csl + 2 * R * 16 + 4 * g) = cs[0]; if (R < 3) *(lf4*)(csl + (2 * R + 1) * 16 + 4 * g) = cs[1]; }
    __syncthreads();
    f32x4 cv[7];
#pragma unroll
    for (int n = 0; n < 7; ++n) cv[n] = *(lf4*)(csl + (n < 6 ? 2 * (((n >> 1) + R) % 3) + (n & 1) : 6) * 16 + 4 * g);
    char* dst = acat + (size_t)((mb0 * KB2 + 32 + 7 * (j >> 1)) * 1024) + lane16;
#pragma unroll
    for (int mi = 0; mi < 4; ++mi) {
      const f32x2 st = stat[R * 64 + mi * 16 + rr];
#pragma unroll
      for (int u = 0; u < 3; ++u) {
        const f32x4 a = nrm(acc[mi][2 * u], cv[2 * u], st), b = nrm(acc[mi][2 * u + 1], cv[2 * u + 1], st);
        const u32x4 o = {relu2(pk2(a[0], a[1])), relu2(pk2(a[2], a[3])), relu2(pk2(b[0], b[1])), relu2(pk2(b[2], b[3]))};
        st128(dst + (mi * KB2 + 4 * fo + (u + R) % 3) * 1024, o);
      }
      const f32x4 a = nrm(acc[mi][6], cv[6], st);
      const u32x2 o = {relu2(pk2(a[0], a[1])), relu2(pk2(a[2], a[3]))};
      st64(dst + (mi * KB2 + 3) * 1024 + fo * 8, o);
      if (mi == 1) BAR();
    }
  } else {
    const char* w1 = uni(w1_);
    issueX(0, 0, 0);
    unsigned wo[4];
#pragma unroll
    for (int k = 0; k < 4; ++k) {
      const int r = R * 16 + k * 4 + g;
      wo[k] = r * 7168 + (j >> 1) * 448 + (j & 1) * 192 + (rr ^ (((r >> 3) & 1) | ((r & 3) << 2))) * 16;
    }
    auto issueW = [&](int t, int B) {
#pragma unroll
      for (int k = 0; k < NW_; ++k) glds(w1, wo[k], tk(t) * 64 * 7168, shm + G1_WH + B * 16384 + (R * 4 + k) * 1024);
    };
    issueW(0, 0); issueX(0, 1, G1_XU); issueW(1, 1);
    SCHED();
    const unsigned lb = l0 + G1_WQ + ((8 * g + q) * 12 + (p ^ (g & 1))) * 16;
    const unsigned wb0 = lb + (p & 1) * 8, wb1 = wb0 ^ 8, wbh = lb + (1 + (R >> 1)) * 64 + (((p & 1) ^ (R & 1)) * 8);
    const unsigned hra = l0 + 32768 + rr * 128 + (g ^ (rr >> 1)) * 16;
    unsigned hr0 = rr ? hra : l0 + G1_ONE, hr1 = rr ? hra ^ 64 : l0 + G1_ONE, hm1 = rr ? 1u : 0u;
    asm volatile("" : "+v"(hr0), "+v"(hr1), "+v"(hm1));
    if (threadIdx.x == 256) *(lu4*)(shm + G1_ONE) = __builtin_bit_cast(u32x4, ones);
    const unsigned wu = blockIdx.x * 4 + R;
    const char* tp; unsigned ts;
    {
      const unsigned n = wu * 17 + min(lane, 16), s = 2 + (n >= 8704u), r = n >= 8704u ? n - 8704u : n;
      tp = r < 3072u ? wqkv_ + s * 393216u + r * 128u : r < 6656u ? w1_ + s * 458752u + (r - 3072u) * 128u : x_ + (r - 6656u) * 2048u + s * 128u;
      ts = r < 3072u ? 786432u : r < 6656u ? 917504u : 256u;
    }
    auto touch = [&](int t) {
      if (t == 7) {
        const unsigned n = min(wu * 11 + min(lane, 10), 10535u);
        tp = n < 8192u ? w2_ + n * 128u : wo_ + (n - 8192u) * 128u;
        ts = n < 8192u ? 8192u * 128u : 2340u * 128u;
      }
      if (lane < 17) asm volatile("global_load_dword %0, %1, off" : "+v"(pfd) : "v"(tp) : "memory");
      tp += ts;
    };
#pragma unroll
    for (int mi = 0; mi < 4; ++mi)
#pragma unroll
      for (int n = 0; n < 6; ++n) { acc[mi][n] = (f32x4){0.f, 0.f, 0.f, 0.f}; asm volatile("" : "+v"(acc[mi][n])); }
    f32x4 hacc = z; asm volatile("" : "+v"(hacc));
    SCHED();
    WAIT_V(8); WAIT_L0(); BAR();
    ST(0, 2);
    BAR();
    SCHED();
    int xc = 0, xn2 = 2 * G1_XU;
    auto half = [&](auto Bc, auto Kc, int t) {
      constexpr int B = decltype(Bc)::value, KH = decltype(Kc)::value, WO = B * 12288 + KH * 6144;
      s16x8 xa[4], hx; s16x4 l[6], h[6], hl, hh;
      const unsigned xp = (KH ? xr1 : xr0) + xc;
      xa[0] = rd128<0>(xp); xa[1] = rd128<2048>(xp); xa[2] = rd128<4096>(xp); xa[3] = rd128<6144>(xp);
      l[0] = tr_read<WO>(wb0); h[0] = tr_read<WO + 768>(wb0); l[1] = tr_read<WO>(wb1); h[1] = tr_read<WO + 768>(wb1);
      l[2] = tr_read<WO + 64>(wb0); h[2] = tr_read<WO + 832>(wb0); l[3] = tr_read<WO + 64>(wb1); h[3] = tr_read<WO + 832>(wb1);
      l[4] = tr_read<WO + 128>(wb0); h[4] = tr_read<WO + 896>(wb0); l[5] = tr_read<WO + 128>(wb1); h[5] = tr_read<WO + 896>(wb1);
      hl = tr_read<WO>(wbh); hh = tr_read<WO + 768>(wbh);
      hx = rd128<0>(__umul24(hm1, xc) + (KH ? hr1 : hr0));
      if (KH == 0) {
        if (t + 2 < G1_NT) {
          issueX(0, t + 2, xn2);
          touch(t);
        }
      } else {
        if (t + 2 < G1_NT) { issueW(t + 2, B); WAIT_V(9); } else WAIT_V(0);
      }
      WAIT_L0(); BAR(); SCHED();
      __builtin_amdgcn_s_setprio(1);
      asm volatile("s_cmp_gt_u32 %5, 1\n\ts_cbranch_scc1 1f\n\ts_cmp_eq_u32 %5, 1\n\ts_cbranch_scc1 2f\n\t" MM "%0, %1, %4, %0\n\ts_branch 3f\n"
                   "1:\n\t" MM "%0, %3, %3, %0\n\ts_branch 3f\n2:\n\t" MM "%0, %2, %4, %0\n3:"
                   : "+v"(cs[0]) : "v"(PK8(l[0], h[0])), "v"(PK8(l[1], h[1])), "v"(hx), "v"(ones), "s"(R) : "scc");
      SCHED();
#pragma unroll
      for (int n = 0; n < 6; ++n) {
        const s16x8 w = PK8(l[n], h[n]);
#pragma unroll
        for (int mi = 0; mi < 4; ++mi) AMMA(acc[mi][n], w, xa[mi]);
      }
      AMMA(hacc, PK8(hl, hh), hx);
#pragma unroll
      for (int i = 0; i < 2; ++i) { AMMA(gs[i], ones, xa[2 + i]); AMMA(gq[i], xa[2 + i], xa[2 + i]); }
      __builtin_amdgcn_s_setprio(0);
      SCHED(); BAR(); SCHED();
    };
    for (int t = 0; t < G1_NT; t += 2) {
      half(IC<0>{}, IC<0>{}, t); half(IC<0>{}, IC<1>{}, t);
      xn2 = xc; xc = xc == 2 * G1_XU ? 0 : xc + G1_XU;
      half(IC<1>{}, IC<0>{}, t + 1); half(IC<1>{}, IC<1>{}, t + 1);
      xn2 = xc; xc = xc == 2 * G1_XU ? 0 : xc + G1_XU;
    }
    asm volatile("" :: "v"(pfd));
    MMA_DONE(); SCHED();
    ST(0, 3);
    put_stats();
    if (rr == 0) { *(lf4*)(csl + (9 + R) * 16 + 4 * g) = hacc; if (R < 2) *(lf4*)(csl + (7 + R) * 16 + 4 * g) = cs[0]; }
    if (R == 2) { if (g == 0) hst[rr] = cs[0][0]; if (g == q) hst[16 + rr] = diag(cs[0]); }
    __syncthreads();
    f32x4 cv[6];
#pragma unroll
    for (int n = 0; n < 6; ++n) cv[n] = *(lf4*)(csl + (7 + n) * 16 + 4 * g);
    u32x4 fr[4][3];
#pragma unroll
    for (int mi = 0; mi < 4; ++mi) {
      const f32x2 st = stat[R * 64 + mi * 16 + rr];
#pragma unroll
      for (int u = 0; u < 3; ++u) {
        const f32x4 a = nrm(acc[mi][2 * u], cv[2 * u], st), c = nrm(acc[mi][2 * u + 1], cv[2 * u + 1], st);
        fr[mi][u] = (u32x4){pk2(a[0], a[1]), pk2(a[2], a[3]), pk2(c[0], c[1]), pk2(c[2], c[3])};
      }
    }
    lchar* vimg = shm + 4096;
    lchar* kblk = shm + 21504;
#pragma unroll
    for (int mi = 0; mi < 4; ++mi) *(lu4*)(vimg + (16 + R * 64 + mi * 16 + rr) * 64 + g * 16) = fr[mi][2];
    if (R < 3) *(lu4*)(kblk + (R + 1) * 1024 + lane16) = fr[3][1];
    {
      const f32x4 a = nrm(hacc, *(lf4*)(csl + (9 + R) * 16 + 4 * g), ln_stat(hst[rr], hst[16 + rr]));
      lchar* d = ((R < 2) ? (kblk + lane16) : (vimg + rr * 64 + g * 16)) + (R & 1) * 8;
      *(lu2*)d = (u32x2){pk2(a[0], a[1]), pk2(a[2], a[3])};
    }
    f32x4 s0[4], s1[4];
#pragma unroll
    for (int mi = 0; mi < 4; ++mi) {
      s1[mi] = MFMA(__builtin_bit_cast(s16x8, fr[mi][1]), __builtin_bit_cast(s16x8, fr[mi][0]), z);
      if (mi) s0[mi] = MFMA(__builtin_bit_cast(s16x8, fr[mi - 1][1]), __builtin_bit_cast(s16x8, fr[mi][0]), z);
    }
    WAIT_L0(); BAR(); SCHED();
    const u32x4 kprev = *(lu4*)(kblk + R * 1024 + lane16);
    s0[0] = MFMA(__builtin_bit_cast(s16x8, kprev), __builtin_bit_cast(s16x8, fr[0][0]), z);
    const unsigned vb = l0 + 4096 + (R * 64 + 4 * g + q) * 64 + p * 16;
    s16x4 va[4], vc[4], vd[4], ve[4];
    va[0] = tr_read<0>(vb); vc[0] = tr_read<8>(vb); vd[0] = tr_read<1024>(vb); ve[0] = tr_read<1032>(vb);
    va[1] = tr_read<1024>(vb); vc[1] = tr_read<1032>(vb); vd[1] = tr_read<2048>(vb); ve[1] = tr_read<2056>(vb);
    va[2] = tr_read<2048>(vb); vc[2] = tr_read<2056>(vb); vd[2] = tr_read<3072>(vb); ve[2] = tr_read<3080>(vb);
    va[3] = tr_read<3072>(vb); vc[3] = tr_read<3080>(vb); vd[3] = tr_read<4096>(vb); ve[3] = tr_read<4104>(vb);
    const int k0 = 4 * g + (rr & 1);
    unsigned par = 0u - (unsigned)(rr & 1), c0 = (unsigned)((k0 - rr - 1) >> 31), c1 = (unsigned)((k0 + 1 - rr) >> 31);
    asm volatile("" : "+v"(par), "+v"(c0), "+v"(c1));
    const float scale = 0.17677669529663687f;
    float e0[4], e1[4], mx[4], sm[4];
#pragma unroll
    for (int mi = 0; mi < 4; ++mi) {
      const float a0 = sel(c0, sel(par, s1[mi][1], s1[mi][0]), sel(par, s0[mi][1], s0[mi][0]));
      const float a1 = sel(c1, sel(par, s1[mi][3], s1[mi][2]), sel(par, s0[mi][3], s0[mi][2]));
      e0[mi] = rbf(rbf(a0) * scale); e1[mi] = rbf(rbf(a1) * scale);
      if (mi == 0) { const unsigned ok = mb0 == 0 ? 0u : ~0u; e0[0] = sel(c0 | ok, e0[0], -1e9f); e1[0] = sel(c1 | ok, e1[0], -1e9f); }
      mx[mi] = fmaxf(e0[mi], e1[mi]);
    }
#pragma unroll
    for (int mi = 0; mi < 4; ++mi) mx[mi] = red16<true>(mx[mi]);
#pragma unroll
    for (int mi = 0; mi < 4; ++mi) mx[mi] = red32<true>(mx[mi]);
#pragma unroll
    for (int mi = 0; mi < 4; ++mi) { e0[mi] = __expf(e0[mi] - mx[mi]); e1[mi] = __expf(e1[mi] - mx[mi]); sm[mi] = e0[mi] + e1[mi]; }
#pragma unroll
    for (int mi = 0; mi < 4; ++mi) sm[mi] = red16<false>(sm[mi]);
#pragma unroll
    for (int mi = 0; mi < 4; ++mi) sm[mi] = red32<false>(sm[mi]);
    WAIT_L0(); SCHED();
    char* dst = acat + (size_t)((mb0 * KB2 + j) * 1024) + lane16;
#pragma unroll
    for (int mi = 0; mi < 4; ++mi) {
      const float inv = __builtin_amdgcn_rcpf(sm[mi]);
      const unsigned pr = pk2(e0[mi] * inv, e1[mi] * inv);
      const unsigned hm = par ^ 0xffffu;
      const unsigned p0 = ((pr << 16) | (pr & 0xffffu)) & hm, p1 = ((pr & 0xffff0000u) | (pr >> 16)) & hm;
      const u32x4 pp = {p0 & ~c0, p1 & ~c1, p0 & c0, p1 & c1};
      const s16x8 pf8 = __builtin_bit_cast(s16x8, pp);
      const f32x4 o0 = MFMA(PK8(va[mi], vd[mi]), pf8, z);
      const f32x4 o1 = MFMA(PK8(vc[mi], ve[mi]), pf8, z);
      const u32x4 o = {pk2(o0[0], o0[1]), pk2(o0[2], o0[3]), pk2(o1[0], o1[1]), pk2(o1[2], o1[3])};
      st128(dst + mi * KB2 * 1024, o);
    }
  }
  ST(0, 4);
  ST(0, 5); ST(0, 6); ST(0, 8); ST(0, 7);
}

#define G2NST 5
constexpr int G2_NST = G2NST, G2_STG = 32768, G2_LDS = G2_NST * G2_STG, G2_NK = 36, G2_PFD = G2_NST + 8;
typedef __attribute__((address_space(1))) unsigned gu32;

__global__ __launch_bounds__(768, 3) void gemm2_kernel(const char* __restrict__ acat, const char* __restrict__ wo_,
                                                       const char* __restrict__ w2_, const char* __restrict__ x,
                                                       char* __restrict__ out, char* __restrict__ exch,
                                                       unsigned* __restrict__ flags) {
  extern __shared__ __attribute__((aligned(16))) char shm_[];
  lchar* shm = (lchar*)shm_;
  ST(1, 0); ST(1, 1);
  const int xcd = blockIdx.x & 7, idx = blockIdx.x >> 3;
  const int ks = xcd & 1, mt = (xcd >> 1) * 4 + (idx & 3), nt = idx >> 2;
  const int wid = __builtin_amdgcn_readfirstlane(threadIdx.x >> 6), lane = threadIdx.x & 63;
  const int wl = wid & 3, G = wid >> 2;
  const unsigned lane16 = lane * 16;
  if (G == 2) {
    const char* A = uni(acat); const char* Wo = uni(wo_); const char* W2 = uni(w2_);
    const unsigned a0 = ((mt * 8 + 2 * wl) * KB2 + ks * 2 * G2_NK) * 1024;
    unsigned wo[4];
#pragma unroll
    for (int k = 0; k < 4; ++k) {
      const int r = wl * 16 + k * 4 + (lane >> 4), pos = lane & 15;
      wo[k] = (r ^ (((r >> 3) & 1) << 2)) * 2048 + (pos ^ (((r >> 3) & 1) | ((r & 3) << 2))) * 16 + nt * 256;
    }
    auto issue = [&](int s, int buf) {
      const int kt = ks * G2_NK + s;
      lchar* dst = shm + buf * G2_STG + wl * 4096;
      const char* Wr = kt < 16 ? Wo : W2;
      const unsigned so = (kt < 16 ? kt : kt - 16) * 131072;
#pragma unroll
      for (int k = 0; k < 4; ++k) {
        glds(A, lane16, a0 + s * 2048 + (k >> 1) * (KB2 * 1024) + (k & 1) * 1024, dst + k * 1024);
        glds(Wr, wo[k], so, dst + 16384 + k * 1024);
      }
    };
    issue(0, 0); issue(1, 1); issue(2, 2);
    WAIT_V(16); BAR();
    issue(3, 3);
    WAIT_V(16); BAR();
    int ib = 4;
    for (int j = 0; j < G2_NK; ++j) {
      if (j + 4 < G2_NK) { issue(j + 4, ib); WAIT_V(16); }
      else if (j + 3 < G2_NK) WAIT_V(8); else WAIT_V(0);
      BAR();
      ib = ib + 1 == G2_NST ? 0 : ib + 1;
    }
    BAR(); BAR(); BAR();
    return;
  }
  const int wr = wl >> 1, wc = wl & 1;
  const int g = lane >> 4, q = (lane >> 2) & 3, p = lane & 3;
  const unsigned l0 = (unsigned)(size_t)shm;
  unsigned ab[4], wb[4];
#pragma unroll
  for (int mi = 0; mi < 4; ++mi) ab[mi] = l0 + wr * 8192 + (mi ^ (2 * G + ks)) * 2048 + lane16;
#pragma unroll
  for (int n = 0; n < 4; ++n)
    wb[n] = l0 + 16384 + ((8 * g + q) * 16 + 4 * ((wc * 2 + (n >> 1)) ^ q) + (p ^ (g & 1))) * 16 + (((p & 1) ^ (n & 1)) * 8);
  f32x4 acc[4][4];
#pragma unroll
  for (int mi = 0; mi < 4; ++mi)
#pragma unroll
    for (int n = 0; n < 4; ++n) { acc[mi][n] = (f32x4){0.f, 0.f, 0.f, 0.f}; asm volatile("" : "+v"(acc[mi][n])); }
  SCHED();
  BAR();
  ST(1, 2);
#define PH(a_, b_)
  if (G == 1) BAR();
  SCHED();
  unsigned bo = G * G2_STG;
  for (int s = G; s < G2_NK; s += 2) {
    s16x8 a[4][2]; s16x4 l[2][4], h[2][4];
    unsigned pa[4], pw[4];
#pragma unroll
    for (int i = 0; i < 4; ++i) { pa[i] = ab[i] + bo; pw[i] = wb[i] + bo; }
#pragma unroll
    for (int i = 0; i < 4; ++i) { a[i][0] = rd128<0>(pa[i]); l[0][i] = tr_read<0>(pw[i]); h[0][i] = tr_read<1024>(pw[i]); }
#pragma unroll
    for (int i = 0; i < 4; ++i) { a[i][1] = rd128<1024>(pa[i]); l[1][i] = tr_read<8192>(pw[i]); h[1][i] = tr_read<9216>(pw[i]); }
    WAIT_L(12); BAR(); SCHED();
    __builtin_amdgcn_s_setprio(1);
#pragma unroll
    for (int kh = 0; kh < 2; ++kh) {
      if (kh) { SCHED(); WAIT_L0(); SCHED(); }
#pragma unroll
      for (int n = 0; n < 4; ++n) {
        const s16x8 w = PK8(l[kh][n], h[kh][n]);
#pragma unroll
        for (int mi = 0; mi < 4; ++mi) acc[mi][n] = MFMA(w, a[mi][kh], acc[mi][n]);
      }
    }
    __builtin_amdgcn_s_setprio(0);
    SCHED(); BAR(); SCHED();
    bo += 2 * G2_STG; if (bo >= G2_NST * G2_STG) bo -= G2_NST * G2_STG;
  }
  if (G == 0) BAR();
  SCHED();
  ST(1, 3);
  const int row = mt * 128 + wr * 64 + (2 * G + ks) * 16 + (lane & 15);
  const size_t ooff = (size_t)row * 2048 + (nt * 128 + wc * 64 + g * 8) * 2;
  const u32x4 xv0 = *(const u32x4*)(x + ooff), xv1 = *(const u32x4*)(x + ooff + 64);
#pragma unroll
  for (int i = 0; i < 2; ++i)
#pragma unroll
    for (int n = 0; n < 4; ++n) *(lf4*)(shm + ((wid * 2 + i) * 4 + n) * 1024 + lane16) = acc[2 + i][n];
  WAIT_L0(); BAR(); SCHED();
  f32x4 keep[4], send[4];
#pragma unroll
  for (int n = 0; n < 4; ++n) {
    send[n] = acc[1][n] + *(lf4*)(shm + (((wid ^ 4) * 2 + 1) * 4 + n) * 1024 + lane16);
    keep[n] = acc[0][n] + *(lf4*)(shm + (((wid ^ 4) * 2 + 0) * 4 + n) * 1024 + lane16);
  }
  const int tile = mt * 8 + nt;
  const rsrc_t rs = mkrs(exch + (size_t)tile * 65536, 65536);
#pragma unroll
  for (int n = 0; n < 4; ++n)
    __builtin_amdgcn_raw_buffer_store_b128(__builtin_bit_cast(u32x4, send[n]), rs, ks * 32768 + (wid * 4 + n) * 1024 + lane16, 0, 16);
  ST(1, 4);
  WAIT_V(0); BAR();
  ST(1, 5);
  if (threadIdx.x == 0) {
    __hip_atomic_store((gu32*)(flags + tile * 2 + ks), 1u, __ATOMIC_RELAXED, __HIP_MEMORY_SCOPE_AGENT);
    while (__hip_atomic_load((gu32*)(flags + tile * 2 + (ks ^ 1)), __ATOMIC_RELAXED, __HIP_MEMORY_SCOPE_AGENT) == 0u) __builtin_amdgcn_s_sleep(1);
  }
  asm volatile("" ::: "memory");
  BAR();
  ST(1, 6);
  asm volatile("" ::: "memory");
#pragma unroll
  for (int n = 0; n < 4; ++n)
    keep[n] += __builtin_bit_cast(f32x4, __builtin_amdgcn_raw_buffer_load_b128(rs, (ks ^ 1) * 32768 + (wid * 4 + n) * 1024 + lane16, 0, 16));
  const bool odd = g & 1;
#pragma unroll
  for (int u = 0; u < 2; ++u) {
    f32x4 lo, hi;
#pragma unroll
    for (int e = 0; e < 4; ++e) { lo[e] = odd ? keep[2 * u + 1][e] : keep[2 * u][e]; hi[e] = odd ? keep[2 * u][e] : keep[2 * u + 1][e]; }
    const u32x4 xv = u ? xv1 : xv0;
    const u32x4 o = {pk2(lo[0] + lo16(xv[0]), lo[1] + hi16(xv[0])), pk2(lo[2] + lo16(xv[1]), lo[3] + hi16(xv[1])),
                     pk2(hi[0] + lo16(xv[2]), hi[1] + hi16(xv[2])), pk2(hi[2] + lo16(xv[3]), hi[3] + hi16(xv[3]))};
    *(u32x4*)(out + ooff + u * 64) = o;
  }
  ST(1, 8); ST(1, 7);
}

extern "C" void kernel_launch(void* const* d_in, const int* in_sizes, int n_in, void* d_out, int out_size,
                              void* d_ws, size_t ws_size, hipStream_t stream) {
  const char* x = (const char*)d_in[0];
  const char* wqkv = (const char*)d_in[1];
  const char* wo = (const char*)d_in[2];
  const char* w1 = (const char*)d_in[4];
  const char* w2 = (const char*)d_in[5];
  char* ws = (char*)d_ws;
  (void)hipFuncSetAttribute((const void*)gemm1_kernel, hipFuncAttributeMaxDynamicSharedMemorySize, G1_LDS);
  (void)hipFuncSetAttribute((const void*)gemm2_kernel, hipFuncAttributeMaxDynamicSharedMemorySize, G2_LDS);
  gemm1_kernel<<<256, 512, G1_LDS, stream>>>(x, wqkv, w1, ws + WS_ACAT, wo, w2, (unsigned*)(ws + WS_FLAG));
  gemm2_kernel<<<256, 768, G2_LDS, stream>>>(ws + WS_ACAT, wo, w2, x, (char*)d_out, ws + WS_EXCH, (unsigned*)(ws + WS_FLAG));
}
```

```cpp
#include <hip/hip_runtime.h>
#include <hip/hip_bf16.h>

typedef short s16x4 __attribute__((ext_vector_type(4)));
typedef short s16x8 __attribute__((ext_vector_type(8)));
typedef __bf16 bf16x2 __attribute__((ext_vector_type(2)));
typedef float f32x4 __attribute__((ext_vector_type(4)));
typedef unsigned u32x4 __attribute__((ext_vector_type(4)));
typedef unsigned u32x2 __attribute__((ext_vector_type(2)));
typedef __attribute__((address_space(3))) char lchar;
typedef __attribute__((address_space(3))) unsigned lu32;

template <int V> struct IC { static constexpr int value = V; };
#define BAR() __builtin_amdgcn_s_barrier()
#define SCHED() __builtin_amdgcn_sched_barrier(0)
#define WAIT_V(n) asm volatile("s_waitcnt vmcnt(%0)" ::"n"(n) : "memory")
#define WAIT_L(n) asm volatile("s_waitcnt lgkmcnt(%0)" ::"n"(n) : "memory")
#define WAIT_L0() asm volatile("s_waitcnt lgkmcnt(0)" ::: "memory")
#define PK8(L, H) (s16x8){L[0], L[1], L[2], L[3], H[0], H[1], H[2], H[3]}
#define MFMA(a, b, c) __builtin_amdgcn_mfma_f32_16x16x32_bf16(a, b, c, 0, 0, 0)

#define ST(k, i)
constexpr int SEQ = 2048, DM = 1024, DFF = 3584, KC = 4608, KB2 = KC / 32;
constexpr size_t WS_XN = 0, WS_ACAT = 4u << 20, WS_EXCH = 24u << 20, WS_FLAG = 40u << 20;

__device__ __forceinline__ unsigned pk2(float a, float b) {
  bf16x2 v = {(__bf16)a, (__bf16)b};
  return __builtin_bit_cast(unsigned, v);
}
__device__ __forceinline__ float rbf(float a) { return (float)(__bf16)a; }
__device__ __forceinline__ float lo16(unsigned u) { return __builtin_bit_cast(float, u << 16); }
__device__ __forceinline__ float hi16(unsigned u) { return __builtin_bit_cast(float, u & 0xffff0000u); }

typedef short s16x2 __attribute__((ext_vector_type(2)));
__device__ __forceinline__ unsigned relu2(unsigned u) {
  const s16x2 z = {0, 0};
  asm("" : "+v"(u));
  return __builtin_bit_cast(unsigned, __builtin_elementwise_max(__builtin_bit_cast(s16x2, u), z));
}
template <bool MAX> __device__ __forceinline__ float red16(float v) {
  const unsigned u = __builtin_bit_cast(unsigned, v);
  auto r = __builtin_amdgcn_permlane16_swap(u, u, false, false);
  const unsigned r0 = r[0], r1 = r[1];
  const float a = __builtin_bit_cast(float, r0), b = __builtin_bit_cast(float, r1);
  return MAX ? fmaxf(a, b) : a + b;
}
template <bool MAX> __device__ __forceinline__ float red32(float v) {
  const unsigned u = __builtin_bit_cast(unsigned, v);
  auto r = __builtin_amdgcn_permlane32_swap(u, u, false, false);
  const unsigned r0 = r[0], r1 = r[1];
  const float a = __builtin_bit_cast(float, r0), b = __builtin_bit_cast(float, r1);
  return MAX ? fmaxf(a, b) : a + b;
}
template <int OFF> __device__ __forceinline__ s16x4 tr_read(unsigned a) {
  s16x4 r; asm volatile("ds_read_b64_tr_b16 %0, %1 offset:%2" : "=&v"(r) : "v"(a), "i"(OFF) : "memory"); return r;
}
template <int OFF> __device__ __forceinline__ s16x8 rd128(unsigned a) {
  s16x8 r; asm volatile("ds_read_b128 %0, %1 offset:%2" : "=&v"(r) : "v"(a), "i"(OFF) : "memory"); return r;
}
typedef __amdgpu_buffer_rsrc_t rsrc_t;
__device__ __forceinline__ rsrc_t mkrs(const void* p, unsigned bytes) {
  return __builtin_amdgcn_make_buffer_rsrc((void*)p, 0, bytes, 0x00020000);
}
__device__ __forceinline__ const char* uni(const char* p) {
  const unsigned long long u = (unsigned long long)p;
  const unsigned lo = __builtin_amdgcn_readfirstlane((unsigned)u), hi = __builtin_amdgcn_readfirstlane((unsigned)(u >> 32));
  return (const char*)(((unsigned long long)hi << 32) | lo);
}
#define NX_ 4
#define NQ_ 3
#define NW_ 4
#define PFL 15
#define PFR 13
#define STPOL ""
__device__ __forceinline__ void st128(char* p, u32x4 v) { asm volatile("global_store_dwordx4 %0, %1, off " STPOL "\n\ts_nop 1" :: "v"(p), "v"(v) : "memory"); }
__device__ __forceinline__ void st64(char* p, u32x2 v) { asm volatile("global_store_dwordx2 %0, %1, off " STPOL "\n\ts_nop 1" :: "v"(p), "v"(v) : "memory"); }
__device__ __forceinline__ void glds(const char* base, unsigned voff, unsigned soff, lchar* l) {
  asm volatile("s_mov_b32 m0, %2\n\ts_nop 0\n\tglobal_load_lds_dwordx4 %0, %1"
               :: "v"(voff), "s"(base + soff), "s"((unsigned)(size_t)l) : "memory");
}

constexpr int G1_XU = 34816, G1_WH = 3 * G1_XU, G1_WQ = G1_WH + 32768, G1_ONE = G1_WQ + 24576, G1_LDS = G1_ONE + 16, G1_NT = 16;
typedef __attribute__((address_space(3))) u32x4 lu4;
typedef __attribute__((address_space(3))) u32x2 lu2;
typedef __attribute__((address_space(3))) f32x4 lf4;
typedef float f32x2 __attribute__((ext_vector_type(2)));
typedef __attribute__((address_space(3))) f32x2 lf2;
typedef __attribute__((address_space(3))) float lf1;

#define MM "v_mfma_f32_16x16x32_bf16 "
#define AMMA(c, a, b) asm volatile(MM "%0, %1, %2, %0" : "+v"(c) : "v"(a), "v"(b))
#define MMA_DONE() asm volatile("s_nop 15\n\ts_nop 15" ::: "memory")
__device__ __forceinline__ float sel(unsigned m, float a, float b) {
  return __builtin_bit_cast(float, (__builtin_bit_cast(unsigned, a) & m) | (__builtin_bit_cast(unsigned, b) & ~m));
}
__device__ __forceinline__ float redg(float v) { return red32<false>(red16<false>(v)); }
__device__ __forceinline__ f32x2 ln_stat(float s, float q) {
  const float mean = s * (1.f / DM), var = fmaxf(q * (1.f / DM) - mean * mean, 0.f);
  return (f32x2){rbf(mean), __builtin_amdgcn_rcpf(rbf(__builtin_amdgcn_sqrtf(rbf(rbf(var) + 1e-5f))))};
}
__device__ __forceinline__ f32x4 nrm(f32x4 a, f32x4 cs, f32x2 st) { return (a - st[0] * cs) * st[1]; }

__global__ __launch_bounds__(512, 2) void gemm1_kernel(const char* __restrict__ x_, const char* __restrict__ wqkv_,
                                                       const char* __restrict__ w1_, char* __restrict__ acat,
                                                       const char* __restrict__ wo_, const char* __restrict__ w2_,
                                                       unsigned* __restrict__ flags) {
  extern __shared__ __attribute__((aligned(16))) char shm_[];
  lchar* shm = (lchar*)shm_;
  ST(0, 0); ST(0, 1);
  const char* pc; asm volatile("s_getpc_b64 %0" : "=s"(pc)); SCHED();
  const int xcd = blockIdx.x & 7, idx = blockIdx.x >> 3;
  const int mt = (xcd & 1) * 4 + (idx & 3), j = (xcd >> 1) * 8 + (idx >> 2);
  const int wid = __builtin_amdgcn_readfirstlane(threadIdx.x >> 6), lane = threadIdx.x & 63;
  const int G = wid >> 2, R = wid & 3;
  const int g = lane >> 4, q = (lane >> 2) & 3, p = lane & 3, rr = lane & 15;
  const unsigned l0 = (unsigned)(size_t)shm, lane16 = lane * 16;
  const char* x = uni(x_);
#define ROT 0
  const int rot = ROT;
  auto tk = [&](int t) { return (t + rot) & (G1_NT - 1); };
  const unsigned xo0 = (lane >> 3) * 2048 + ((lane & 7) ^ g) * 16, xo1 = (lane >> 3) * 2048 + ((lane & 7) ^ (4 + g)) * 16;
  auto issueX = [&](int h, int t, int xbuf) {
#pragma unroll
    for (int a = 0; a < NX_; ++a)
      glds(x, (a & 1) ? xo1 : xo0, (mt * 256 + h * 128 + R * 32 + a * 8) * 2048 + tk(t) * 128, shm + xbuf + (h * 16 + R * 4 + a) * 1024);
  };
  const unsigned xr0 = l0 + (R * 64 + rr) * 128 + (g ^ (rr >> 1)) * 16, xr1 = xr0 ^ 64;
  f32x4 acc[4][7];
  const f32x4 z = {0.f, 0.f, 0.f, 0.f};
  f32x4 gs[2] = {z, z}, gq[2] = {z, z}, cs[2] = {z, z};
  s16x8 ones = {0x3f80, 0x3f80, 0x3f80, 0x3f80, 0x3f80, 0x3f80, 0x3f80, 0x3f80};
  asm volatile("" : "+v"(ones), "+v"(gs[0]), "+v"(gs[1]), "+v"(gq[0]), "+v"(gq[1]), "+v"(cs[0]), "+v"(cs[1]));
  lf2* stat = (lf2*)shm;
  lf1* csl = (lf1*)(shm + 2304);
  lf1* hst = (lf1*)(shm + 3584);
  auto diag = [&](f32x4 d) { const unsigned m1 = 0u - (rr & 1), m2 = 0u - ((rr >> 1) & 1); return sel(m2, sel(m1, d[3], d[2]), sel(m1, d[1], d[0])); };
  auto put_stats = [&]() {
    if (g == q) { stat[R * 64 + G * 32 + rr] = ln_stat(gs[0][0], diag(gq[0])); stat[R * 64 + G * 32 + 16 + rr] = ln_stat(gs[1][0], diag(gq[1])); }
  };
  const int fo = j & 1;
  const int mb0 = mt * 16 + R * 4;
  unsigned pfd = 0;

  if (G == 0) {
    const char* wq = uni(wqkv_);
    if (threadIdx.x < 148) asm volatile("global_load_dword %0, %1, %2" : "+v"(pfd) : "v"(threadIdx.x * 128), "s"(pc) : "memory");
    issueX(1, 0, 0);
    SCHED();
    unsigned wo[3];
#pragma unroll
    for (int k = 0; k < 3; ++k) {
      const int i = (3 * R + k) * 64 + lane, r = i / 12, ch = (i % 12) ^ ((r >> 3) & 1);
      wo[k] = r * 6144 + (ch >> 2) * 2048 + j * 64 + (ch & 3) * 16;
    }
    const int hrow = mt > 0 ? mt * 256 - 16 : 0;
    auto issueQ = [&](int t, int B) {
#pragma unroll
      for (int k = 0; k < NQ_; ++k) glds(wq, wo[k], tk(t) * 64 * 6144, shm + G1_WQ + B * 12288 + (3 * R + k) * 1024);
    };
    auto issueH = [&](int t, int xbuf) {
      if (R < 2) glds(x, R ? xo1 : xo0, (hrow + R * 8) * 2048 + tk(t) * 128, shm + xbuf + (32 + R) * 1024);
    };
    issueQ(0, 0); issueH(0, 0); issueH(1, G1_XU); issueX(1, 1, G1_XU);
    SCHED();
    if (threadIdx.x == 0) flags[blockIdx.x] = 0u;
    unsigned wb[7];
#pragma unroll
    for (int n = 0; n < 7; ++n) {
      const int F = n < 6 ? ((n >> 1) + R) % 3 + fo : (fo ? 0 : 3), f = n < 6 ? (n & 1) : fo;
      wb[n] = l0 + G1_WH + ((8 * g + q) * 16 + 4 * (F ^ q) + (p ^ (g & 1))) * 16 + (((p & 1) ^ f) * 8);
    }
#pragma unroll
    for (int mi = 0; mi < 4; ++mi)
#pragma unroll
      for (int n = 0; n < 7; ++n) { acc[mi][n] = (f32x4){0.f, 0.f, 0.f, 0.f}; asm volatile("" : "+v"(acc[mi][n])); }
    SCHED();
    WAIT_V(7); BAR();
    ST(0, 2);
    SCHED();
    int xc = 0, xn2 = 2 * G1_XU;
    auto half = [&](auto Bc, auto Kc, int t) {
      constexpr int B = decltype(Bc)::value, KH = decltype(Kc)::value;
      s16x8 xa[4]; s16x4 l[7], h[7];
      const unsigned xp = (KH ? xr1 : xr0) + xc;
      xa[0] = rd128<0>(xp); xa[1] = rd128<2048>(xp); xa[2] = rd128<4096>(xp); xa[3] = rd128<6144>(xp);
#pragma unroll
      for (int n = 0; n < 7; ++n) { l[n] = tr_read<B * 16384 + KH * 8192>(wb[n]); h[n] = tr_read<B * 16384 + KH * 8192 + 1024>(wb[n]); }
      if (KH == 0) {
        if (t + 1 < G1_NT) { issueQ(t + 1, B ^ 1); if (t + 2 < G1_NT) issueH(t + 2, xn2); WAIT_V(7); } else WAIT_V(0);
      } else {
        if (t + 2 < G1_NT) { issueX(1, t + 2, xn2); WAIT_V(7); } else WAIT_V(3);
      }
      WAIT_L0(); BAR(); SCHED();
      __builtin_amdgcn_s_setprio(1);
      asm volatile("s_cmp_eq_u32 %6, 3\n\ts_cbranch_scc1 1f\n\t" MM "%0, %2, %5, %0\n\t" MM "%1, %3, %5, %1\n\ts_branch 2f\n"
                   "1:\n\t" MM "%0, %4, %5, %0\n2:"
                   : "+v"(cs[0]), "+v"(cs[1]) : "v"(PK8(l[0], h[0])), "v"(PK8(l[1], h[1])), "v"(PK8(l[6], h[6])), "v"(ones), "s"(R) : "scc");
      SCHED();
#pragma unroll
      for (int n = 0; n < 7; ++n) {
        const s16x8 w = PK8(l[n], h[n]);
#pragma unroll
        for (int mi = 0; mi < 4; ++mi) AMMA(acc[mi][n], w, xa[mi]);
      }
#pragma unroll
      for (int i = 0; i < 2; ++i) { AMMA(gs[i], ones, xa[i]); AMMA(gq[i], xa[i], xa[i]); }
      __builtin_amdgcn_s_setprio(0);
      SCHED(); BAR(); SCHED();
    };
    for (int t = 0; t < G1_NT; t += 2) {
      half(IC<0>{}, IC<0>{}, t); half(IC<0>{}, IC<1>{}, t);
      xn2 = xc; xc = xc == 2 * G1_XU ? 0 : xc + G1_XU;
      half(IC<1>{}, IC<0>{}, t + 1); half(IC<1>{}, IC<1>{}, t + 1);
      xn2 = xc; xc = xc == 2 * G1_XU ? 0 : xc + G1_XU;
    }
    BAR(); MMA_DONE(); SCHED();
    asm volatile("" :: "v"(pfd));
    ST(0, 3);
    put_stats();
    if (rr == 0) { *(lf4*)(csl + 2 * R * 16 + 4 * g) = cs[0]; if (R < 3) *(lf4*)(csl + (2 * R + 1) * 16 + 4 * g) = cs[1]; }
    __syncthreads();
    f32x4 cv[7];
#pragma unroll
    for (int n = 0; n < 7; ++n) cv[n] = *(lf4*)(csl + (n < 6 ? 2 * (((n >> 1) + R) % 3) + (n & 1) : 6) * 16 + 4 * g);
    char* dst = acat + (size_t)((mb0 * KB2 + 32 + 7 * (j >> 1)) * 1024) + lane16;
#pragma unroll
    for (int mi = 0; mi < 4; ++mi) {
      const f32x2 st = stat[R * 64 + mi * 16 + rr];
#pragma unroll
      for (int u = 0; u < 3; ++u) {
        const f32x4 a = nrm(acc[mi][2 * u], cv[2 * u], st), b = nrm(acc[mi][2 * u + 1], cv[2 * u + 1], st);
        const u32x4 o = {relu2(pk2(a[0], a[1])), relu2(pk2(a[2], a[3])), relu2(pk2(b[0], b[1])), relu2(pk2(b[2], b[3]))};
        st128(dst + (mi * KB2 + 4 * fo + (u + R) % 3) * 1024, o);
      }
      const f32x4 a = nrm(acc[mi][6], cv[6], st);
      const u32x2 o = {relu2(pk2(a[0], a[1])), relu2(pk2(a[2], a[3]))};
      st64(dst + (mi * KB2 + 3) * 1024 + fo * 8, o);
    }
  } else {
    const char* w1 = uni(w1_);
    issueX(0, 0, 0);
    unsigned wo[4];
#pragma unroll
    for (int k = 0; k < 4; ++k) {
      const int r = R * 16 + k * 4 + g;
      wo[k] = r * 7168 + (j >> 1) * 448 + (j & 1) * 192 + (rr ^ (((r >> 3) & 1) | ((r & 3) << 2))) * 16;
    }
    auto issueW = [&](int t, int B) {
#pragma unroll
      for (int k = 0; k < NW_; ++k) glds(w1, wo[k], tk(t) * 64 * 7168, shm + G1_WH + B * 16384 + (R * 4 + k) * 1024);
    };
    issueW(0, 0); issueX(0, 1, G1_XU); issueW(1, 1);
    SCHED();
    const unsigned lb = l0 + G1_WQ + ((8 * g + q) * 12 + (p ^ (g & 1))) * 16;
    const unsigned wb0 = lb + (p & 1) * 8, wb1 = wb0 ^ 8, wbh = lb + (1 + (R >> 1)) * 64 + (((p & 1) ^ (R & 1)) * 8);
    const unsigned hra = l0 + 32768 + rr * 128 + (g ^ (rr >> 1)) * 16;
    unsigned hr0 = rr ? hra : l0 + G1_ONE, hr1 = rr ? hra ^ 64 : l0 + G1_ONE, hm1 = rr ? 1u : 0u;
    asm volatile("" : "+v"(hr0), "+v"(hr1), "+v"(hm1));
    if (threadIdx.x == 256) *(lu4*)(shm + G1_ONE) = __builtin_bit_cast(u32x4, ones);
    const unsigned wu = blockIdx.x * 4 + R;
    const char* tp; unsigned ts;
    {
      const unsigned n = wu * 17 + min(lane, 16), s = 2 + (n >= 8704u), r = n >= 8704u ? n - 8704u : n;
      tp = r < 3072u ? wqkv_ + s * 393216u + r * 128u : r < 6656u ? w1_ + s * 458752u + (r - 3072u) * 128u : x_ + (r - 6656u) * 2048u + s * 128u;
      ts = r < 3072u ? 786432u : r < 6656u ? 917504u : 256u;
    }
    auto touch = [&](int t) {
      if (t == 7) {
        const unsigned n = min(wu * 11 + min(lane, 10), 10535u);
        tp = n < 8192u ? w2_ + n * 128u : wo_ + (n - 8192u) * 128u;
        ts = n < 8192u ? 8192u * 128u : 2340u * 128u;
      }
      if (lane < 17) asm volatile("global_load_dword %0, %1, off" : "+v"(pfd) : "v"(tp) : "memory");
      tp += ts;
    };
#pragma unroll
    for (int mi = 0; mi < 4; ++mi)
#pragma unroll
      for (int n = 0; n < 6; ++n) { acc[mi][n] = (f32x4){0.f, 0.f, 0.f, 0.f}; asm volatile("" : "+v"(acc[mi][n])); }
    f32x4 hacc = z; asm volatile("" : "+v"(hacc));
    SCHED();
    WAIT_V(8); WAIT_L0(); BAR();
    ST(0, 2);
    BAR();
    SCHED();
    int xc = 0, xn2 = 2 * G1_XU;
    auto half = [&](auto Bc, auto Kc, int t) {
      constexpr int B = decltype(Bc)::value, KH = decltype(Kc)::value, WO = B * 12288 + KH * 6144;
      s16x8 xa[4], hx; s16x4 l[6], h[6], hl, hh;
      const unsigned xp = (KH ? xr1 : xr0) + xc;
      xa[0] = rd128<0>(xp); xa[1] = rd128<2048>(xp); xa[2] = rd128<4096>(xp); xa[3] = rd128<6144>(xp);
      l[0] = tr_read<WO>(wb0); h[0] = tr_read<WO + 768>(wb0); l[1] = tr_read<WO>(wb1); h[1] = tr_read<WO + 768>(wb1);
      l[2] = tr_read<WO + 64>(wb0); h[2] = tr_read<WO + 832>(wb0); l[3] = tr_read<WO + 64>(wb1); h[3] = tr_read<WO + 832>(wb1);
      l[4] = tr_read<WO + 128>(wb0); h[4] = tr_read<WO + 896>(wb0); l[5] = tr_read<WO + 128>(wb1); h[5] = tr_read<WO + 896>(wb1);
      hl = tr_read<WO>(wbh); hh = tr_read<WO + 768>(wbh);
      hx = rd128<0>(__umul24(hm1, xc) + (KH ? hr1 : hr0));
      if (KH == 0) {
        if (t + 2 < G1_NT) {
          issueX(0, t + 2, xn2);
          touch(t);
        }
      } else {
        if (t + 2 < G1_NT) { issueW(t + 2, B); WAIT_V(9); } else WAIT_V(0);
      }
      WAIT_L0(); BAR(); SCHED();
      __builtin_amdgcn_s_setprio(1);
      asm volatile("s_cmp_gt_u32 %5, 1\n\ts_cbranch_scc1 1f\n\ts_cmp_eq_u32 %5, 1\n\ts_cbranch_scc1 2f\n\t" MM "%0, %1, %4, %0\n\ts_branch 3f\n"
                   "1:\n\t" MM "%0, %3, %3, %0\n\ts_branch 3f\n2:\n\t" MM "%0, %2, %4, %0\n3:"
                   : "+v"(cs[0]) : "v"(PK8(l[0], h[0])), "v"(PK8(l[1], h[1])), "v"(hx), "v"(ones), "s"(R) : "scc");
      SCHED();
#pragma unroll
      for (int n = 0; n < 6; ++n) {
        const s16x8 w = PK8(l[n], h[n]);
#pragma unroll
        for (int mi = 0; mi < 4; ++mi) AMMA(acc[mi][n], w, xa[mi]);
      }
      AMMA(hacc, PK8(hl, hh), hx);
#pragma unroll
      for (int i = 0; i < 2; ++i) { AMMA(gs[i], ones, xa[2 + i]); AMMA(gq[i], xa[2 + i], xa[2 + i]); }
      __builtin_amdgcn_s_setprio(0);
      SCHED(); BAR(); SCHED();
    };
    for (int t = 0; t < G1_NT; t += 2) {
      half(IC<0>{}, IC<0>{}, t); half(IC<0>{}, IC<1>{}, t);
      xn2 = xc; xc = xc == 2 * G1_XU ? 0 : xc + G1_XU;
      half(IC<1>{}, IC<0>{}, t + 1); half(IC<1>{}, IC<1>{}, t + 1);
      xn2 = xc; xc = xc == 2 * G1_XU ? 0 : xc + G1_XU;
    }
    asm volatile("" :: "v"(pfd));
    MMA_DONE(); SCHED();
    __builtin_amdgcn_s_setprio(2);
    ST(0, 3);
    if (threadIdx.x == 256) *(lu32*)(shm + 3712) = 0u;
    put_stats();
    if (rr == 0) { *(lf4*)(csl + (9 + R) * 16 + 4 * g) = hacc; if (R < 2) *(lf4*)(csl + (7 + R) * 16 + 4 * g) = cs[0]; }
    if (R == 2) { if (g == 0) hst[rr] = cs[0][0]; if (g == q) hst[16 + rr] = diag(cs[0]); }
    __syncthreads();
    f32x4 cv[6];
#pragma unroll
    for (int n = 0; n < 6; ++n) cv[n] = *(lf4*)(csl + (7 + n) * 16 + 4 * g);
    u32x4 fr[4][3];
#pragma unroll
    for (int mi = 0; mi < 4; ++mi) {
      const f32x2 st = stat[R * 64 + mi * 16 + rr];
#pragma unroll
      for (int u = 0; u < 3; ++u) {
        const f32x4 a = nrm(acc[mi][2 * u], cv[2 * u], st), c = nrm(acc[mi][2 * u + 1], cv[2 * u + 1], st);
        fr[mi][u] = (u32x4){pk2(a[0], a[1]), pk2(a[2], a[3]), pk2(c[0], c[1]), pk2(c[2], c[3])};
      }
    }
    lchar* vimg = shm + 4096;
    lchar* kblk = shm + 21504;
#pragma unroll
    for (int mi = 0; mi < 4; ++mi) *(lu4*)(vimg + (16 + R * 64 + mi * 16 + rr) * 64 + g * 16) = fr[mi][2];
    if (R < 3) *(lu4*)(kblk + (R + 1) * 1024 + lane16) = fr[3][1];
    {
      const f32x4 a = nrm(hacc, *(lf4*)(csl + (9 + R) * 16 + 4 * g), ln_stat(hst[rr], hst[16 + rr]));
      lchar* d = ((R < 2) ? (kblk + lane16) : (vimg + rr * 64 + g * 16)) + (R & 1) * 8;
      *(lu2*)d = (u32x2){pk2(a[0], a[1]), pk2(a[2], a[3])};
    }
    f32x4 s0[4], s1[4];
#pragma unroll
    for (int mi = 0; mi < 4; ++mi) {
      s1[mi] = MFMA(__builtin_bit_cast(s16x8, fr[mi][1]), __builtin_bit_cast(s16x8, fr[mi][0]), z);
      if (mi) s0[mi] = MFMA(__builtin_bit_cast(s16x8, fr[mi - 1][1]), __builtin_bit_cast(s16x8, fr[mi][0]), z);
    }
    WAIT_L0();
    if (lane == 0) {
      __hip_atomic_fetch_add((lu32*)(shm + 3712), 1u, __ATOMIC_RELAXED, __HIP_MEMORY_SCOPE_WORKGROUP);
      while (__hip_atomic_load((lu32*)(shm + 3712), __ATOMIC_RELAXED, __HIP_MEMORY_SCOPE_WORKGROUP) < 4u) __builtin_amdgcn_s_sleep(1);
    }
    asm volatile("" ::: "memory");
    SCHED();
    const u32x4 kprev = *(lu4*)(kblk + R * 1024 + lane16);
    s0[0] = MFMA(__builtin_bit_cast(s16x8, kprev), __builtin_bit_cast(s16x8, fr[0][0]), z);
    const unsigned vb = l0 + 4096 + (R * 64 + 4 * g + q) * 64 + p * 16;
    s16x4 va[4], vc[4], vd[4], ve[4];
    va[0] = tr_read<0>(vb); vc[0] = tr_read<8>(vb); vd[0] = tr_read<1024>(vb); ve[0] = tr_read<1032>(vb);
    va[1] = tr_read<1024>(vb); vc[1] = tr_read<1032>(vb); vd[1] = tr_read<2048>(vb); ve[1] = tr_read<2056>(vb);
    va[2] = tr_read<2048>(vb); vc[2] = tr_read<2056>(vb); vd[2] = tr_read<3072>(vb); ve[2] = tr_read<3080>(vb);
    va[3] = tr_read<3072>(vb); vc[3] = tr_read<3080>(vb); vd[3] = tr_read<4096>(vb); ve[3] = tr_read<4104>(vb);
    const int k0 = 4 * g + (rr & 1);
    unsigned par = 0u - (unsigned)(rr & 1), c0 = (unsigned)((k0 - rr - 1) >> 31), c1 = (unsigned)((k0 + 1 - rr) >> 31);
    asm volatile("" : "+v"(par), "+v"(c0), "+v"(c1));
    const float scale = 0.17677669529663687f;
    float e0[4], e1[4], mx[4], sm[4];
#pragma unroll
    for (int mi = 0; mi < 4; ++mi) {
      const float a0 = sel(c0, sel(par, s1[mi][1], s1[mi][0]), sel(par, s0[mi][1], s0[mi][0]));
      const float a1 = sel(c1, sel(par, s1[mi][3], s1[mi][2]), sel(par, s0[mi][3], s0[mi][2]));
      e0[mi] = rbf(rbf(a0) * scale); e1[mi] = rbf(rbf(a1) * scale);
      if (mi == 0) { const unsigned ok = mb0 == 0 ? 0u : ~0u; e0[0] = sel(c0 | ok, e0[0], -1e9f); e1[0] = sel(c1 | ok, e1[0], -1e9f); }
      mx[mi] = fmaxf(e0[mi], e1[mi]);
    }
#pragma unroll
    for (int mi = 0; mi < 4; ++mi) mx[mi] = red16<true>(mx[mi]);
#pragma unroll
    for (int mi = 0; mi < 4; ++mi) mx[mi] = red32<true>(mx[mi]);
#pragma unroll
    for (int mi = 0; mi < 4; ++mi) { e0[mi] = __expf(e0[mi] - mx[mi]); e1[mi] = __expf(e1[mi] - mx[mi]); sm[mi] = e0[mi] + e1[mi]; }
#pragma unroll
    for (int mi = 0; mi < 4; ++mi) sm[mi] = red16<false>(sm[mi]);
#pragma unroll
    for (int mi = 0; mi < 4; ++mi) sm[mi] = red32<false>(sm[mi]);
    WAIT_L0(); SCHED();
    char* dst = acat + (size_t)((mb0 * KB2 + j) * 1024) + lane16;
#pragma unroll
    for (int mi = 0; mi < 4; ++mi) {
      const float inv = __builtin_amdgcn_rcpf(sm[mi]);
      const unsigned pr = pk2(e0[mi] * inv, e1[mi] * inv);
      const unsigned hm = par ^ 0xffffu;
      const unsigned p0 = ((pr << 16) | (pr & 0xffffu)) & hm, p1 = ((pr & 0xffff0000u) | (pr >> 16)) & hm;
      const u32x4 pp = {p0 & ~c0, p1 & ~c1, p0 & c0, p1 & c1};
      const s16x8 pf8 = __builtin_bit_cast(s16x8, pp);
      const f32x4 o0 = MFMA(PK8(va[mi], vd[mi]), pf8, z);
      const f32x4 o1 = MFMA(PK8(vc[mi], ve[mi]), pf8, z);
      const u32x4 o = {pk2(o0[0], o0[1]), pk2(o0[2], o0[3]), pk2(o1[0], o1[1]), pk2(o1[2], o1[3])};
      st128(dst + mi * KB2 * 1024, o);
    }
  }
  ST(0, 4);
  ST(0, 5); ST(0, 6); ST(0, 8); ST(0, 7);
}

#define G2NST 5
constexpr int G2_NST = G2NST, G2_STG = 32768, G2_LDS = G2_NST * G2_STG, G2_NK = 36, G2_PFD = G2_NST + 8;
typedef __attribute__((address_space(1))) unsigned gu32;

__global__ __launch_bounds__(768, 3) void gemm2_kernel(const char* __restrict__ acat, const char* __restrict__ wo_,
                                                       const char* __restrict__ w2_, const char* __restrict__ x,
                                                       char* __restrict__ out, char* __restrict__ exch,
                                                       unsigned* __restrict__ flags) {
  extern __shared__ __attribute__((aligned(16))) char shm_[];
  lchar* shm = (lchar*)shm_;
  ST(1, 0); ST(1, 1);
  const char* pc; asm volatile("s_getpc_b64 %0" : "=s"(pc)); SCHED();
  const int xcd = blockIdx.x & 7, idx = blockIdx.x >> 3;
  const int ks = xcd & 1, mt = (xcd >> 1) * 4 + (idx & 3), nt = idx >> 2;
  const int wid = __builtin_amdgcn_readfirstlane(threadIdx.x >> 6), lane = threadIdx.x & 63;
  const int wl = wid & 3, G = wid >> 2;
  const unsigned lane16 = lane * 16;
  if (G == 2) {
    const char* A = uni(acat); const char* Wo = uni(wo_); const char* W2 = uni(w2_);
    const unsigned a0 = ((mt * 8 + 2 * wl) * KB2 + ks * 2 * G2_NK) * 1024;
    unsigned wo[4];
#pragma unroll
    for (int k = 0; k < 4; ++k) {
      const int r = wl * 16 + k * 4 + (lane >> 4), pos = lane & 15;
      wo[k] = (r ^ (((r >> 3) & 1) << 2)) * 2048 + (pos ^ (((r >> 3) & 1) | ((r & 3) << 2))) * 16 + nt * 256;
    }
    auto issue = [&](int s, int buf) {
      const int kt = ks * G2_NK + s;
      lchar* dst = shm + buf * G2_STG + wl * 4096;
      const char* Wr = kt < 16 ? Wo : W2;
      const unsigned so = (kt < 16 ? kt : kt - 16) * 131072;
#pragma unroll
      for (int k = 0; k < 4; ++k) {
        glds(A, lane16, a0 + s * 2048 + (k >> 1) * (KB2 * 1024) + (k & 1) * 1024, dst + k * 1024);
        glds(Wr, wo[k], so, dst + 16384 + k * 1024);
      }
    };
    issue(0, 0); issue(1, 1); issue(2, 2);
    WAIT_V(16); BAR();
    issue(3, 3);
    WAIT_V(16); BAR();
    int ib = 4;
    for (int j = 0; j < G2_NK; ++j) {
      if (j + 4 < G2_NK) { issue(j + 4, ib); WAIT_V(16); }
      else if (j + 3 < G2_NK) WAIT_V(8); else WAIT_V(0);
      BAR();
      ib = ib + 1 == G2_NST ? 0 : ib + 1;
    }
    BAR(); BAR(); BAR();
    return;
  }
  const int wr = wl >> 1, wc = wl & 1;
  const int g = lane >> 4, q = (lane >> 2) & 3, p = lane & 3;
  const unsigned l0 = (unsigned)(size_t)shm;
  unsigned ab[4], wb[4];
#pragma unroll
  for (int mi = 0; mi < 4; ++mi) ab[mi] = l0 + wr * 8192 + (mi ^ (2 * G + ks)) * 2048 + lane16;
#pragma unroll
  for (int n = 0; n < 4; ++n)
    wb[n] = l0 + 16384 + ((8 * g + q) * 16 + 4 * ((wc * 2 + (n >> 1)) ^ q) + (p ^ (g & 1))) * 16 + (((p & 1) ^ (n & 1)) * 8);
  unsigned pfd = 0;
  if (threadIdx.x < 28) asm volatile("global_load_dword %0, %1, %2" : "+v"(pfd) : "v"(threadIdx.x * 128), "s"(pc) : "memory");
  f32x4 acc[4][4];
#pragma unroll
  for (int mi = 0; mi < 4; ++mi)
#pragma unroll
    for (int n = 0; n < 4; ++n) { acc[mi][n] = (f32x4){0.f, 0.f, 0.f, 0.f}; asm volatile("" : "+v"(acc[mi][n])); }
  SCHED();
  BAR();
  ST(1, 2);
#define PH(a_, b_)
  if (G == 1) BAR();
  SCHED();
  unsigned bo = G * G2_STG;
  for (int s = G; s < G2_NK; s += 2) {
    s16x8 a[4][2]; s16x4 l[2][4], h[2][4];
    unsigned pa[4], pw[4];
#pragma unroll
    for (int i = 0; i < 4; ++i) { pa[i] = ab[i] + bo; pw[i] = wb[i] + bo; }
#pragma unroll
    for (int i = 0; i < 4; ++i) { a[i][0] = rd128<0>(pa[i]); l[0][i] = tr_read<0>(pw[i]); h[0][i] = tr_read<1024>(pw[i]); }
#pragma unroll
    for (int i = 0; i < 4; ++i) { a[i][1] = rd128<1024>(pa[i]); l[1][i] = tr_read<8192>(pw[i]); h[1][i] = tr_read<9216>(pw[i]); }
    WAIT_L(12); BAR(); SCHED();
    __builtin_amdgcn_s_setprio(1);
#pragma unroll
    for (int kh = 0; kh < 2; ++kh) {
      if (kh) { SCHED(); WAIT_L0(); SCHED(); }
#pragma unroll
      for (int n = 0; n < 4; ++n) {
        const s16x8 w = PK8(l[kh][n], h[kh][n]);
#pragma unroll
        for (int mi = 0; mi < 4; ++mi) acc[mi][n] = MFMA(w, a[mi][kh], acc[mi][n]);
      }
    }
    __builtin_amdgcn_s_setprio(0);
    SCHED(); BAR(); SCHED();
    bo += 2 * G2_STG; if (bo >= G2_NST * G2_STG) bo -= G2_NST * G2_STG;
  }
  if (G == 0) BAR();
  SCHED();
  ST(1, 3);
  const int row = mt * 128 + wr * 64 + (2 * G + ks) * 16 + (lane & 15);
  const size_t ooff = (size_t)row * 2048 + (nt * 128 + wc * 64 + g * 8) * 2;
  const u32x4 xv0 = *(const u32x4*)(x + ooff), xv1 = *(const u32x4*)(x + ooff + 64);
#pragma unroll
  for (int i = 0; i < 2; ++i)
#pragma unroll
    for (int n = 0; n < 4; ++n) *(lf4*)(shm + ((wid * 2 + i) * 4 + n) * 1024 + lane16) = acc[2 + i][n];
  WAIT_L0(); BAR(); SCHED();
  f32x4 keep[4], send[4];
#pragma unroll
  for (int n = 0; n < 4; ++n) {
    send[n] = acc[1][n] + *(lf4*)(shm + (((wid ^ 4) * 2 + 1) * 4 + n) * 1024 + lane16);
    keep[n] = acc[0][n] + *(lf4*)(shm + (((wid ^ 4) * 2 + 0) * 4 + n) * 1024 + lane16);
  }
  const int tile = mt * 8 + nt;
  const rsrc_t rs = mkrs(exch + (size_t)tile * 65536, 65536);
#pragma unroll
  for (int n = 0; n < 4; ++n)
    __builtin_amdgcn_raw_buffer_store_b128(__builtin_bit_cast(u32x4, send[n]), rs, ks * 32768 + (wid * 4 + n) * 1024 + lane16, 0, 16);
  ST(1, 4);
  WAIT_V(0); BAR();
  asm volatile("" :: "v"(pfd));
  ST(1, 5);
  if (threadIdx.x == 0) {
    __hip_atomic_store((gu32*)(flags + tile * 2 + ks), 1u, __ATOMIC_RELAXED, __HIP_MEMORY_SCOPE_AGENT);
    while (__hip_atomic_load((gu32*)(flags + tile * 2 + (ks ^ 1)), __ATOMIC_RELAXED, __HIP_MEMORY_SCOPE_AGENT) == 0u) __builtin_amdgcn_s_sleep(1);
  }
  asm volatile("" ::: "memory");
  BAR();
  ST(1, 6);
  asm volatile("" ::: "memory");
#pragma unroll
  for (int n = 0; n < 4; ++n)
    keep[n] += __builtin_bit_cast(f32x4, __builtin_amdgcn_raw_buffer_load_b128(rs, (ks ^ 1) * 32768 + (wid * 4 + n) * 1024 + lane16, 0, 16));
  const bool odd = g & 1;
#pragma unroll
  for (int u = 0; u < 2; ++u) {
    f32x4 lo, hi;
#pragma unroll
    for (int e = 0; e < 4; ++e) { lo[e] = odd ? keep[2 * u + 1][e] : keep[2 * u][e]; hi[e] = odd ? keep[2 * u][e] : keep[2 * u + 1][e]; }
    const u32x4 xv = u ? xv1 : xv0;
    const u32x4 o = {pk2(lo[0] + lo16(xv[0]), lo[1] + hi16(xv[0])), pk2(lo[2] + lo16(xv[1]), lo[3] + hi16(xv[1])),
                     pk2(hi[0] + lo16(xv[2]), hi[1] + hi16(xv[2])), pk2(hi[2] + lo16(xv[3]), hi[3] + hi16(xv[3]))};
    *(u32x4*)(out + ooff + u * 64) = o;
  }
  ST(1, 8); ST(1, 7);
}

extern "C" void kernel_launch(void* const* d_in, const int* in_sizes, int n_in, void* d_out, int out_size,
                              void* d_ws, size_t ws_size, hipStream_t stream) {
  const char* x = (const char*)d_in[0];
  const char* wqkv = (const char*)d_in[1];
  const char* wo = (const char*)d_in[2];
  const char* w1 = (const char*)d_in[4];
  const char* w2 = (const char*)d_in[5];
  char* ws = (char*)d_ws;
  (void)hipFuncSetAttribute((const void*)gemm1_kernel, hipFuncAttributeMaxDynamicSharedMemorySize, G1_LDS);
  (void)hipFuncSetAttribute((const void*)gemm2_kernel, hipFuncAttributeMaxDynamicSharedMemorySize, G2_LDS);
  gemm1_kernel<<<256, 512, G1_LDS, stream>>>(x, wqkv, w1, ws + WS_ACAT, wo, w2, (unsigned*)(ws + WS_FLAG));
  gemm2_kernel<<<256, 768, G2_LDS, stream>>>(ws + WS_ACAT, wo, w2, x, (char*)d_out, ws + WS_EXCH, (unsigned*)(ws + WS_FLAG));
}
```

```cpp
#include <hip/hip_runtime.h>
#include <hip/hip_bf16.h>

typedef short s16x4 __attribute__((ext_vector_type(4)));
typedef short s16x8 __attribute__((ext_vector_type(8)));
typedef __bf16 bf16x2 __attribute__((ext_vector_type(2)));
typedef float f32x4 __attribute__((ext_vector_type(4)));
typedef unsigned u32x4 __attribute__((ext_vector_type(4)));
typedef unsigned u32x2 __attribute__((ext_vector_type(2)));
typedef __attribute__((address_space(3))) char lchar;
typedef __attribute__((address_space(3))) unsigned lu32;

template <int V> struct IC { static constexpr int value = V; };
#define BAR() __builtin_amdgcn_s_barrier()
#define SCHED() __builtin_amdgcn_sched_barrier(0)
#define WAIT_V(n) asm volatile("s_waitcnt vmcnt(%0)" ::"n"(n) : "memory")
#define WAIT_L(n) asm volatile("s_waitcnt lgkmcnt(%0)" ::"n"(n) : "memory")
#define WAIT_L0() asm volatile("s_waitcnt lgkmcnt(0)" ::: "memory")
#define PK8(L, H) (s16x8){L[0], L[1], L[2], L[3], H[0], H[1], H[2], H[3]}
#define MFMA(a, b, c) __builtin_amdgcn_mfma_f32_16x16x32_bf16(a, b, c, 0, 0, 0)

#define ST(k, i)
constexpr int SEQ = 2048, DM = 1024, DFF = 3584, KC = 4608, KB2 = KC / 32;
constexpr size_t WS_XN = 0, WS_ACAT = 4u << 20, WS_EXCH = 24u << 20, WS_FLAG = 40u << 20;

__device__ __forceinline__ unsigned pk2(float a, float b) {
  bf16x2 v = {(__bf16)a, (__bf16)b};
  return __builtin_bit_cast(unsigned, v);
}
__device__ __forceinline__ float rbf(float a) { return (float)(__bf16)a; }
__device__ __forceinline__ float lo16(unsigned u) { return __builtin_bit_cast(float, u << 16); }
__device__ __forceinline__ float hi16(unsigned u) { return __builtin_bit_cast(float, u & 0xffff0000u); }

typedef short s16x2 __attribute__((ext_vector_type(2)));
__device__ __forceinline__ unsigned relu2(unsigned u) {
  const s16x2 z = {0, 0};
  asm("" : "+v"(u));
  return __builtin_bit_cast(unsigned, __builtin_elementwise_max(__builtin_bit_cast(s16x2, u), z));
}
template <bool MAX> __device__ __forceinline__ float red16(float v) {
  const unsigned u = __builtin_bit_cast(unsigned, v);
  auto r = __builtin_amdgcn_permlane16_swap(u, u, false, false);
  const unsigned r0 = r[0], r1 = r[1];
  const float a = __builtin_bit_cast(float, r0), b = __builtin_bit_cast(float, r1);
  return MAX ? fmaxf(a, b) : a + b;
}
template <bool MAX> __device__ __forceinline__ float red32(float v) {
  const unsigned u = __builtin_bit_cast(unsigned, v);
  auto r = __builtin_amdgcn_permlane32_swap(u, u, false, false);
  const unsigned r0 = r[0], r1 = r[1];
  const float a = __builtin_bit_cast(float, r0), b = __builtin_bit_cast(float, r1);
  return MAX ? fmaxf(a, b) : a + b;
}
template <int OFF> __device__ __forceinline__ s16x4 tr_read(unsigned a) {
  s16x4 r; asm volatile("ds_read_b64_tr_b16 %0, %1 offset:%2" : "=&v"(r) : "v"(a), "i"(OFF) : "memory"); return r;
}
template <int OFF> __device__ __forceinline__ s16x8 rd128(unsigned a) {
  s16x8 r; asm volatile("ds_read_b128 %0, %1 offset:%2" : "=&v"(r) : "v"(a), "i"(OFF) : "memory"); return r;
}
typedef __amdgpu_buffer_rsrc_t rsrc_t;
__device__ __forceinline__ rsrc_t mkrs(const void* p, unsigned bytes) {
  return __builtin_amdgcn_make_buffer_rsrc((void*)p, 0, bytes, 0x00020000);
}
__device__ __forceinline__ const char* uni(const char* p) {
  const unsigned long long u = (unsigned long long)p;
  const unsigned lo = __builtin_amdgcn_readfirstlane((unsigned)u), hi = __builtin_amdgcn_readfirstlane((unsigned)(u >> 32));
  return (const char*)(((unsigned long long)hi << 32) | lo);
}
#define NX_ 4
#define NQ_ 3
#define NW_ 4
#define PFL 15
#define PFR 13
#define STPOL ""
__device__ __forceinline__ void st128(char* p, u32x4 v) { asm volatile("global_store_dwordx4 %0, %1, off " STPOL "\n\ts_nop 1" :: "v"(p), "v"(v) : "memory"); }
__device__ __forceinline__ void st64(char* p, u32x2 v) { asm volatile("global_store_dwordx2 %0, %1, off " STPOL "\n\ts_nop 1" :: "v"(p), "v"(v) : "memory"); }
__device__ __forceinline__ void glds(const char* base, unsigned voff, unsigned soff, lchar* l) {
  asm volatile("s_mov_b32 m0, %2\n\ts_nop 0\n\tglobal_load_lds_dwordx4 %0, %1"
               :: "v"(voff), "s"(base + soff), "s"((unsigned)(size_t)l) : "memory");
}

constexpr int G1_XU = 34816, G1_WH = 3 * G1_XU, G1_WQ = G1_WH + 32768, G1_ONE = G1_WQ + 24576, G1_LDS = G1_ONE + 16, G1_NT = 16;
typedef __attribute__((address_space(3))) u32x4 lu4;
typedef __attribute__((address_space(3))) u32x2 lu2;
typedef __attribute__((address_space(3))) f32x4 lf4;
typedef float f32x2 __attribute__((ext_vector_type(2)));
typedef __attribute__((address_space(3))) f32x2 lf2;
typedef __attribute__((address_space(3))) float lf1;

#define MM "v_mfma_f32_16x16x32_bf16 "
#define AMMA(c, a, b) asm volatile(MM "%0, %1, %2, %0" : "+v"(c) : "v"(a), "v"(b))
#define MMA_DONE() asm volatile("s_nop 15\n\ts_nop 15" ::: "memory")
__device__ __forceinline__ float sel(unsigned m, float a, float b) {
  return __builtin_bit_cast(float, (__builtin_bit_cast(unsigned, a) & m) | (__builtin_bit_cast(unsigned, b) & ~m));
}
__device__ __forceinline__ float redg(float v) { return red32<false>(red16<false>(v)); }
__device__ __forceinline__ f32x2 ln_stat(float s, float q) {
  const float mean = s * (1.f / DM), var = fmaxf(q * (1.f / DM) - mean * mean, 0.f);
  return (f32x2){rbf(mean), __builtin_amdgcn_rcpf(rbf(__builtin_amdgcn_sqrtf(rbf(rbf(var) + 1e-5f))))};
}
__device__ __forceinline__ f32x4 nrm(f32x4 a, f32x4 cs, f32x2 st) { return (a - st[0] * cs) * st[1]; }

__global__ __launch_bounds__(512, 2) void gemm1_kernel(const char* __restrict__ x_, const char* __restrict__ wqkv_,
                                                       const char* __restrict__ w1_, char* __restrict__ acat,
                                                       const char* __restrict__ wo_, const char* __restrict__ w2_,
                                                       unsigned* __restrict__ flags) {
  extern __shared__ __attribute__((aligned(16))) char shm_[];
  lchar* shm = (lchar*)shm_;
  ST(0, 0); ST(0, 1);
  const int xcd = blockIdx.x & 7, idx = blockIdx.x >> 3;
  const int mt = (xcd & 1) * 4 + (idx & 3), j = (xcd >> 1) * 8 + (idx >> 2);
  const int wid = __builtin_amdgcn_readfirstlane(threadIdx.x >> 6), lane = threadIdx.x & 63;
  const int G = wid >> 2, R = wid & 3;
  const int g = lane >> 4, q = (lane >> 2) & 3, p = lane & 3, rr = lane & 15;
  const unsigned l0 = (unsigned)(size_t)shm, lane16 = lane * 16;
  const char* x = uni(x_);
#define ROT 0
  const int rot = ROT;
  auto tk = [&](int t) { return (t + rot) & (G1_NT - 1); };
  const unsigned xo0 = (lane >> 3) * 2048 + ((lane & 7) ^ g) * 16, xo1 = (lane >> 3) * 2048 + ((lane & 7) ^ (4 + g)) * 16;
  auto issueX = [&](int h, int t, int xbuf) {
#pragma unroll
    for (int a = 0; a < NX_; ++a)
      glds(x, (a & 1) ? xo1 : xo0, (mt * 256 + h * 128 + R * 32 + a * 8) * 2048 + tk(t) * 128, shm + xbuf + (h * 16 + R * 4 + a) * 1024);
  };
  const unsigned xr0 = l0 + (R * 64 + rr) * 128 + (g ^ (rr >> 1)) * 16, xr1 = xr0 ^ 64;
  f32x4 acc[4][7];
  const f32x4 z = {0.f, 0.f, 0.f, 0.f};
  f32x4 gs[2] = {z, z}, gq[2] = {z, z}, cs[2] = {z, z};
  s16x8 ones = {0x3f80, 0x3f80, 0x3f80, 0x3f80, 0x3f80, 0x3f80, 0x3f80, 0x3f80};
  asm volatile("" : "+v"(ones), "+v"(gs[0]), "+v"(gs[1]), "+v"(gq[0]), "+v"(gq[1]), "+v"(cs[0]), "+v"(cs[1]));
  lf2* stat = (lf2*)shm;
  lf1* csl = (lf1*)(shm + 2304);
  lf1* hst = (lf1*)(shm + 3584);
  auto diag = [&](f32x4 d) { const unsigned m1 = 0u - (rr & 1), m2 = 0u - ((rr >> 1) & 1); return sel(m2, sel(m1, d[3], d[2]), sel(m1, d[1], d[0])); };
  auto put_stats = [&]() {
    if (g == q) { stat[R * 64 + G * 32 + rr] = ln_stat(gs[0][0], diag(gq[0])); stat[R * 64 + G * 32 + 16 + rr] = ln_stat(gs[1][0], diag(gq[1])); }
  };
  const int fo = j & 1;
  const int mb0 = mt * 16 + R * 4;
  unsigned pfd = 0;

  if (G == 0) {
    const char* wq = uni(wqkv_);
    issueX(1, 0, 0);
    SCHED();
    unsigned wo[3];
#pragma unroll
    for (int k = 0; k < 3; ++k) {
      const int i = (3 * R + k) * 64 + lane, r = i / 12, ch = (i % 12) ^ ((r >> 3) & 1);
      wo[k] = r * 6144 + (ch >> 2) * 2048 + j * 64 + (ch & 3) * 16;
    }
    const int hrow = mt > 0 ? mt * 256 - 16 : 0;
    auto issueQ = [&](int t, int B) {
#pragma unroll
      for (int k = 0; k < NQ_; ++k) glds(wq, wo[k], tk(t) * 64 * 6144, shm + G1_WQ + B * 12288 + (3 * R + k) * 1024);
    };
    auto issueH = [&](int t, int xbuf) {
      if (R < 2) glds(x, R ? xo1 : xo0, (hrow + R * 8) * 2048 + tk(t) * 128, shm + xbuf + (32 + R) * 1024);
    };
    issueQ(0, 0); issueH(0, 0); issueH(1, G1_XU); issueX(1, 1, G1_XU);
    SCHED();
    if (threadIdx.x < 8) flags[(blockIdx.x * 8 + threadIdx.x) * 32] = 0u;
    unsigned wb[7];
#pragma unroll
    for (int n = 0; n < 7; ++n) {
      const int F = n < 6 ? ((n >> 1) + R) % 3 + fo : (fo ? 0 : 3), f = n < 6 ? (n & 1) : fo;
      wb[n] = l0 + G1_WH + ((8 * g + q) * 16 + 4 * (F ^ q) + (p ^ (g & 1))) * 16 + (((p & 1) ^ f) * 8);
    }
#pragma unroll
    for (int mi = 0; mi < 4; ++mi)
#pragma unroll
      for (int n = 0; n < 7; ++n) { acc[mi][n] = (f32x4){0.f, 0.f, 0.f, 0.f}; asm volatile("" : "+v"(acc[mi][n])); }
    SCHED();
    WAIT_V(7); BAR();
    ST(0, 2);
    SCHED();
    int xc = 0, xn2 = 2 * G1_XU;
    auto half = [&](auto Bc, auto Kc, int t) {
      constexpr int B = decltype(Bc)::value, KH = decltype(Kc)::value;
      s16x8 xa[4]; s16x4 l[7], h[7];
      const unsigned xp = (KH ? xr1 : xr0) + xc;
      xa[0] = rd128<0>(xp); xa[1] = rd128<2048>(xp); xa[2] = rd128<4096>(xp); xa[3] = rd128<6144>(xp);
#pragma unroll
      for (int n = 0; n < 7; ++n) { l[n] = tr_read<B * 16384 + KH * 8192>(wb[n]); h[n] = tr_read<B * 16384 + KH * 8192 + 1024>(wb[n]); }
      if (KH == 0) {
        if (t + 1 < G1_NT) { issueQ(t + 1, B ^ 1); if (t + 2 < G1_NT) issueH(t + 2, xn2); WAIT_V(7); } else WAIT_V(0);
      } else {
        if (t + 2 < G1_NT) { issueX(1, t + 2, xn2); WAIT_V(7); } else WAIT_V(3);
      }
      WAIT_L0(); BAR(); SCHED();
      __builtin_amdgcn_s_setprio(1);
      asm volatile("s_cmp_eq_u32 %6, 3\n\ts_cbranch_scc1 1f\n\t" MM "%0, %2, %5, %0\n\t" MM "%1, %3, %5, %1\n\ts_branch 2f\n"
                   "1:\n\t" MM "%0, %4, %5, %0\n2:"
                   : "+v"(cs[0]), "+v"(cs[1]) : "v"(PK8(l[0], h[0])), "v"(PK8(l[1], h[1])), "v"(PK8(l[6], h[6])), "v"(ones), "s"(R) : "scc");
      SCHED();
#pragma unroll
      for (int n = 0; n < 7; ++n) {
        const s16x8 w = PK8(l[n], h[n]);
#pragma unroll
        for (int mi = 0; mi < 4; ++mi) AMMA(acc[mi][n], w, xa[mi]);
      }
#pragma unroll
      for (int i = 0; i < 2; ++i) { AMMA(gs[i], ones, xa[i]); AMMA(gq[i], xa[i], xa[i]); }
      __builtin_amdgcn_s_setprio(0);
      SCHED(); BAR(); SCHED();
    };
    for (int t = 0; t < G1_NT; t += 2) {
      half(IC<0>{}, IC<0>{}, t); half(IC<0>{}, IC<1>{}, t);
      xn2 = xc; xc = xc == 2 * G1_XU ? 0 : xc + G1_XU;
      half(IC<1>{}, IC<0>{}, t + 1); half(IC<1>{}, IC<1>{}, t + 1);
      xn2 = xc; xc = xc == 2 * G1_XU ? 0 : xc + G1_XU;
    }
    BAR(); MMA_DONE(); SCHED();
    ST(0, 3);
    put_stats();
    if (rr == 0) { *(lf4*)(csl + 2 * R * 16 + 4 * g) = cs[0]; if (R < 3) *(lf4*)(csl + (2 * R + 1) * 16 + 4 * g) = cs[1]; }
    __syncthreads();
    f32x4 cv[7];
#pragma unroll
    for (int n = 0; n < 7; ++n) cv[n] = *(lf4*)(csl + (n < 6 ? 2 * (((n >> 1) + R) % 3) + (n & 1) : 6) * 16 + 4 * g);
    char* dst = acat + (size_t)((mb0 * KB2 + 32 + 7 * (j >> 1)) * 1024) + lane16;
#pragma unroll
    for (int mi = 0; mi < 4; ++mi) {
      const f32x2 st = stat[R * 64 + mi * 16 + rr];
#pragma unroll
      for (int u = 0; u < 3; ++u) {
        const f32x4 a = nrm(acc[mi][2 * u], cv[2 * u], st), b = nrm(acc[mi][2 * u + 1], cv[2 * u + 1], st);
        const u32x4 o = {relu2(pk2(a[0], a[1])), relu2(pk2(a[2], a[3])), relu2(pk2(b[0], b[1])), relu2(pk2(b[2], b[3]))};
        st128(dst + (mi * KB2 + 4 * fo + (u + R) % 3) * 1024, o);
      }
      const f32x4 a = nrm(acc[mi][6], cv[6], st);
      const u32x2 o = {relu2(pk2(a[0], a[1])), relu2(pk2(a[2], a[3]))};
      st64(dst + (mi * KB2 + 3) * 1024 + fo * 8, o);
    }
  } else {
    const char* w1 = uni(w1_);
    issueX(0, 0, 0);
    unsigned wo[4];
#pragma unroll
    for (int k = 0; k < 4; ++k) {
      const int r = R * 16 + k * 4 + g;
      wo[k] = r * 7168 + (j >> 1) * 448 + (j & 1) * 192 + (rr ^ (((r >> 3) & 1) | ((r & 3) << 2))) * 16;
    }
    auto issueW = [&](int t, int B) {
#pragma unroll
      for (int k = 0; k < NW_; ++k) glds(w1, wo[k], tk(t) * 64 * 7168, shm + G1_WH + B * 16384 + (R * 4 + k) * 1024);
    };
    issueW(0, 0); issueX(0, 1, G1_XU); issueW(1, 1);
    SCHED();
    const unsigned lb = l0 + G1_WQ + ((8 * g + q) * 12 + (p ^ (g & 1))) * 16;
    const unsigned wb0 = lb + (p & 1) * 8, wb1 = wb0 ^ 8, wbh = lb + (1 + (R >> 1)) * 64 + (((p & 1) ^ (R & 1)) * 8);
    const unsigned hra = l0 + 32768 + rr * 128 + (g ^ (rr >> 1)) * 16;
    unsigned hr0 = rr ? hra : l0 + G1_ONE, hr1 = rr ? hra ^ 64 : l0 + G1_ONE, hm1 = rr ? 1u : 0u;
    asm volatile("" : "+v"(hr0), "+v"(hr1), "+v"(hm1));
    if (threadIdx.x == 256) *(lu4*)(shm + G1_ONE) = __builtin_bit_cast(u32x4, ones);
    const unsigned wu = blockIdx.x * 4 + R;
    const char* tp; unsigned ts;
    {
      const unsigned n = wu * 17 + min(lane, 16), s = 2 + (n >= 8704u), r = n >= 8704u ? n - 8704u : n;
      tp = r < 3072u ? wqkv_ + s * 393216u + r * 128u : r < 6656u ? w1_ + s * 458752u + (r - 3072u) * 128u : x_ + (r - 6656u) * 2048u + s * 128u;
      ts = r < 3072u ? 786432u : r < 6656u ? 917504u : 256u;
    }
    auto touch = [&](int t) {
      if (t == 7) {
        const unsigned n = min(wu * 11 + min(lane, 10), 10535u);
        tp = n < 8192u ? w2_ + n * 128u : wo_ + (n - 8192u) * 128u;
        ts = n < 8192u ? 8192u * 128u : 2340u * 128u;
      }
      if (lane < 17) asm volatile("global_load_dword %0, %1, off" : "+v"(pfd) : "v"(tp) : "memory");
      tp += ts;
    };
#pragma unroll
    for (int mi = 0; mi < 4; ++mi)
#pragma unroll
      for (int n = 0; n < 6; ++n) { acc[mi][n] = (f32x4){0.f, 0.f, 0.f, 0.f}; asm volatile("" : "+v"(acc[mi][n])); }
    f32x4 hacc = z; asm volatile("" : "+v"(hacc));
    SCHED();
    WAIT_V(8); WAIT_L0(); BAR();
    ST(0, 2);
    BAR();
    SCHED();
    int xc = 0, xn2 = 2 * G1_XU;
    auto half = [&](auto Bc, auto Kc, int t) {
      constexpr int B = decltype(Bc)::value, KH = decltype(Kc)::value, WO = B * 12288 + KH * 6144;
      s16x8 xa[4], hx; s16x4 l[6], h[6], hl, hh;
      const unsigned xp = (KH ? xr1 : xr0) + xc;
      xa[0] = rd128<0>(xp); xa[1] = rd128<2048>(xp); xa[2] = rd128<4096>(xp); xa[3] = rd128<6144>(xp);
      l[0] = tr_read<WO>(wb0); h[0] = tr_read<WO + 768>(wb0); l[1] = tr_read<WO>(wb1); h[1] = tr_read<WO + 768>(wb1);
      l[2] = tr_read<WO + 64>(wb0); h[2] = tr_read<WO + 832>(wb0); l[3] = tr_read<WO + 64>(wb1); h[3] = tr_read<WO + 832>(wb1);
      l[4] = tr_read<WO + 128>(wb0); h[4] = tr_read<WO + 896>(wb0); l[5] = tr_read<WO + 128>(wb1); h[5] = tr_read<WO + 896>(wb1);
      hl = tr_read<WO>(wbh); hh = tr_read<WO + 768>(wbh);
      hx = rd128<0>(__umul24(hm1, xc) + (KH ? hr1 : hr0));
      if (KH == 0) {
        if (t + 2 < G1_NT) {
          issueX(0, t + 2, xn2);
          touch(t);
        }
      } else {
        if (t + 2 < G1_NT) { issueW(t + 2, B); WAIT_V(9); } else WAIT_V(0);
      }
      WAIT_L0(); BAR(); SCHED();
      __builtin_amdgcn_s_setprio(1);
      asm volatile("s_cmp_gt_u32 %5, 1\n\ts_cbranch_scc1 1f\n\ts_cmp_eq_u32 %5, 1\n\ts_cbranch_scc1 2f\n\t" MM "%0, %1, %4, %0\n\ts_branch 3f\n"
                   "1:\n\t" MM "%0, %3, %3, %0\n\ts_branch 3f\n2:\n\t" MM "%0, %2, %4, %0\n3:"
                   : "+v"(cs[0]) : "v"(PK8(l[0], h[0])), "v"(PK8(l[1], h[1])), "v"(hx), "v"(ones), "s"(R) : "scc");
      SCHED();
#pragma unroll
      for (int n = 0; n < 6; ++n) {
        const s16x8 w = PK8(l[n], h[n]);
#pragma unroll
        for (int mi = 0; mi < 4; ++mi) AMMA(acc[mi][n], w, xa[mi]);
      }
      AMMA(hacc, PK8(hl, hh), hx);
#pragma unroll
      for (int i = 0; i < 2; ++i) { AMMA(gs[i], ones, xa[2 + i]); AMMA(gq[i], xa[2 + i], xa[2 + i]); }
      __builtin_amdgcn_s_setprio(0);
      SCHED(); BAR(); SCHED();
    };
    for (int t = 0; t < G1_NT; t += 2) {
      half(IC<0>{}, IC<0>{}, t); half(IC<0>{}, IC<1>{}, t);
      xn2 = xc; xc = xc == 2 * G1_XU ? 0 : xc + G1_XU;
      half(IC<1>{}, IC<0>{}, t + 1); half(IC<1>{}, IC<1>{}, t + 1);
      xn2 = xc; xc = xc == 2 * G1_XU ? 0 : xc + G1_XU;
    }
    asm volatile("" :: "v"(pfd));
    MMA_DONE(); SCHED();
    __builtin_amdgcn_s_setprio(2);
    ST(0, 3);
    if (threadIdx.x == 256) *(lu32*)(shm + 3712) = 0u;
    put_stats();
    if (rr == 0) { *(lf4*)(csl + (9 + R) * 16 + 4 * g) = hacc; if (R < 2) *(lf4*)(csl + (7 + R) * 16 + 4 * g) = cs[0]; }
    if (R == 2) { if (g == 0) hst[rr] = cs[0][0]; if (g == q) hst[16 + rr] = diag(cs[0]); }
    __syncthreads();
    f32x4 cv[6];
#pragma unroll
    for (int n = 0; n < 6; ++n) cv[n] = *(lf4*)(csl + (7 + n) * 16 + 4 * g);
    u32x4 fr[4][3];
#pragma unroll
    for (int mi = 0; mi < 4; ++mi) {
      const f32x2 st = stat[R * 64 + mi * 16 + rr];
#pragma unroll
      for (int u = 0; u < 3; ++u) {
        const f32x4 a = nrm(acc[mi][2 * u], cv[2 * u], st), c = nrm(acc[mi][2 * u + 1], cv[2 * u + 1], st);
        fr[mi][u] = (u32x4){pk2(a[0], a[1]), pk2(a[2], a[3]), pk2(c[0], c[1]), pk2(c[2], c[3])};
      }
    }
    lchar* vimg = shm + 4096;
    lchar* kblk = shm + 21504;
#pragma unroll
    for (int mi = 0; mi < 4; ++mi) *(lu4*)(vimg + (16 + R * 64 + mi * 16 + rr) * 64 + g * 16) = fr[mi][2];
    if (R < 3) *(lu4*)(kblk + (R + 1) * 1024 + lane16) = fr[3][1];
    {
      const f32x4 a = nrm(hacc, *(lf4*)(csl + (9 + R) * 16 + 4 * g), ln_stat(hst[rr], hst[16 + rr]));
      lchar* d = ((R < 2) ? (kblk + lane16) : (vimg + rr * 64 + g * 16)) + (R & 1) * 8;
      *(lu2*)d = (u32x2){pk2(a[0], a[1]), pk2(a[2], a[3])};
    }
    f32x4 s0[4], s1[4];
#pragma unroll
    for (int mi = 0; mi < 4; ++mi) {
      s1[mi] = MFMA(__builtin_bit_cast(s16x8, fr[mi][1]), __builtin_bit_cast(s16x8, fr[mi][0]), z);
      if (mi) s0[mi] = MFMA(__builtin_bit_cast(s16x8, fr[mi - 1][1]), __builtin_bit_cast(s16x8, fr[mi][0]), z);
    }
    WAIT_L0();
    if (lane == 0) {
      __hip_atomic_fetch_add((lu32*)(shm + 3712), 1u, __ATOMIC_RELAXED, __HIP_MEMORY_SCOPE_WORKGROUP);
      while (__hip_atomic_load((lu32*)(shm + 3712), __ATOMIC_RELAXED, __HIP_MEMORY_SCOPE_WORKGROUP) < 4u) __builtin_amdgcn_s_sleep(1);
    }
    asm volatile("" ::: "memory");
    SCHED();
    const u32x4 kprev = *(lu4*)(kblk + R * 1024 + lane16);
    s0[0] = MFMA(__builtin_bit_cast(s16x8, kprev), __builtin_bit_cast(s16x8, fr[0][0]), z);
    const unsigned vb = l0 + 4096 + (R * 64 + 4 * g + q) * 64 + p * 16;
    s16x4 va[4], vc[4], vd[4], ve[4];
    va[0] = tr_read<0>(vb); vc[0] = tr_read<8>(vb); vd[0] = tr_read<1024>(vb); ve[0] = tr_read<1032>(vb);
    va[1] = tr_read<1024>(vb); vc[1] = tr_read<1032>(vb); vd[1] = tr_read<2048>(vb); ve[1] = tr_read<2056>(vb);
    va[2] = tr_read<2048>(vb); vc[2] = tr_read<2056>(vb); vd[2] = tr_read<3072>(vb); ve[2] = tr_read<3080>(vb);
    va[3] = tr_read<3072>(vb); vc[3] = tr_read<3080>(vb); vd[3] = tr_read<4096>(vb); ve[3] = tr_read<4104>(vb);
    const int k0 = 4 * g + (rr & 1);
    unsigned par = 0u - (unsigned)(rr & 1), c0 = (unsigned)((k0 - rr - 1) >> 31), c1 = (unsigned)((k0 + 1 - rr) >> 31);
    asm volatile("" : "+v"(par), "+v"(c0), "+v"(c1));
    const float scale = 0.17677669529663687f;
    float e0[4], e1[4], mx[4], sm[4];
#pragma unroll
    for (int mi = 0; mi < 4; ++mi) {
      const float a0 = sel(c0, sel(par, s1[mi][1], s1[mi][0]), sel(par, s0[mi][1], s0[mi][0]));
      const float a1 = sel(c1, sel(par, s1[mi][3], s1[mi][2]), sel(par, s0[mi][3], s0[mi][2]));
      e0[mi] = rbf(rbf(a0) * scale); e1[mi] = rbf(rbf(a1) * scale);
      if (mi == 0) { const unsigned ok = mb0 == 0 ? 0u : ~0u; e0[0] = sel(c0 | ok, e0[0], -1e9f); e1[0] = sel(c1 | ok, e1[0], -1e9f); }
      mx[mi] = fmaxf(e0[mi], e1[mi]);
    }
#pragma unroll
    for (int mi = 0; mi < 4; ++mi) mx[mi] = red16<true>(mx[mi]);
#pragma unroll
    for (int mi = 0; mi < 4; ++mi) mx[mi] = red32<true>(mx[mi]);
#pragma unroll
    for (int mi = 0; mi < 4; ++mi) { e0[mi] = __expf(e0[mi] - mx[mi]); e1[mi] = __expf(e1[mi] - mx[mi]); sm[mi] = e0[mi] + e1[mi]; }
#pragma unroll
    for (int mi = 0; mi < 4; ++mi) sm[mi] = red16<false>(sm[mi]);
#pragma unroll
    for (int mi = 0; mi < 4; ++mi) sm[mi] = red32<false>(sm[mi]);
    WAIT_L0(); SCHED();
    char* dst = acat + (size_t)((mb0 * KB2 + j) * 1024) + lane16;
#pragma unroll
    for (int mi = 0; mi < 4; ++mi) {
      const float inv = __builtin_amdgcn_rcpf(sm[mi]);
      const unsigned pr = pk2(e0[mi] * inv, e1[mi] * inv);
      const unsigned hm = par ^ 0xffffu;
      const unsigned p0 = ((pr << 16) | (pr & 0xffffu)) & hm, p1 = ((pr & 0xffff0000u) | (pr >> 16)) & hm;
      const u32x4 pp = {p0 & ~c0, p1 & ~c1, p0 & c0, p1 & c1};
      const s16x8 pf8 = __builtin_bit_cast(s16x8, pp);
      const f32x4 o0 = MFMA(PK8(va[mi], vd[mi]), pf8, z);
      const f32x4 o1 = MFMA(PK8(vc[mi], ve[mi]), pf8, z);
      const u32x4 o = {pk2(o0[0], o0[1]), pk2(o0[2], o0[3]), pk2(o1[0], o1[1]), pk2(o1[2], o1[3])};
      st128(dst + mi * KB2 * 1024, o);
    }
  }
  ST(0, 4);
  ST(0, 5); ST(0, 6); ST(0, 8); ST(0, 7);
}

#define G2NST 5
constexpr int G2_NST = G2NST, G2_STG = 32768, G2_LDS = G2_NST * G2_STG, G2_NK = 36, G2_PFD = G2_NST + 8;
typedef __attribute__((address_space(1))) unsigned gu32;

__global__ __launch_bounds__(768, 3) void gemm2_kernel(const char* __restrict__ acat, const char* __restrict__ wo_,
                                                       const char* __restrict__ w2_, const char* __restrict__ x,
                                                       char* __restrict__ out, char* __restrict__ exch,
                                                       unsigned* __restrict__ flags) {
  extern __shared__ __attribute__((aligned(16))) char shm_[];
  lchar* shm = (lchar*)shm_;
  ST(1, 0); ST(1, 1);
  const int xcd = blockIdx.x & 7, idx = blockIdx.x >> 3;
  const int ks = xcd & 1, mt = (xcd >> 1) * 4 + (idx & 3), nt = idx >> 2;
  const int wid = __builtin_amdgcn_readfirstlane(threadIdx.x >> 6), lane = threadIdx.x & 63;
  const int wl = wid & 3, G = wid >> 2;
  const unsigned lane16 = lane * 16;
  if (G == 2) {
    const char* A = uni(acat); const char* Wo = uni(wo_); const char* W2 = uni(w2_);
    const unsigned a0 = ((mt * 8 + 2 * wl) * KB2 + ks * 2 * G2_NK) * 1024;
    unsigned wo[4];
#pragma unroll
    for (int k = 0; k < 4; ++k) {
      const int r = wl * 16 + k * 4 + (lane >> 4), pos = lane & 15;
      wo[k] = (r ^ (((r >> 3) & 1) << 2)) * 2048 + (pos ^ (((r >> 3) & 1) | ((r & 3) << 2))) * 16 + nt * 256;
    }
    auto issue = [&](int s, int buf) {
      const int kt = ks * G2_NK + s;
      lchar* dst = shm + buf * G2_STG + wl * 4096;
      const char* Wr = kt < 16 ? Wo : W2;
      const unsigned so = (kt < 16 ? kt : kt - 16) * 131072;
#pragma unroll
      for (int k = 0; k < 4; ++k) {
        glds(A, lane16, a0 + s * 2048 + (k >> 1) * (KB2 * 1024) + (k & 1) * 1024, dst + k * 1024);
        glds(Wr, wo[k], so, dst + 16384 + k * 1024);
      }
    };
    issue(0, 0); issue(1, 1); issue(2, 2);
    WAIT_V(16); BAR();
    issue(3, 3);
    WAIT_V(16); BAR();
    int ib = 4;
    for (int j = 0; j < G2_NK; ++j) {
      if (j + 4 < G2_NK) { issue(j + 4, ib); WAIT_V(16); }
      else if (j + 3 < G2_NK) WAIT_V(8); else WAIT_V(0);
      BAR();
      ib = ib + 1 == G2_NST ? 0 : ib + 1;
    }
    BAR();
    return;
  }
  const int wr = wl >> 1, wc = wl & 1;
  const int g = lane >> 4, q = (lane >> 2) & 3, p = lane & 3;
  const unsigned l0 = (unsigned)(size_t)shm;
  unsigned ab[4], wb[4];
#pragma unroll
  for (int mi = 0; mi < 4; ++mi) ab[mi] = l0 + wr * 8192 + (mi ^ (2 * G + ks)) * 2048 + lane16;
#pragma unroll
  for (int n = 0; n < 4; ++n)
    wb[n] = l0 + 16384 + ((8 * g + q) * 16 + 4 * ((wc * 2 + (n >> 1)) ^ q) + (p ^ (g & 1))) * 16 + (((p & 1) ^ (n & 1)) * 8);
  f32x4 acc[4][4];
#pragma unroll
  for (int mi = 0; mi < 4; ++mi)
#pragma unroll
    for (int n = 0; n < 4; ++n) { acc[mi][n] = (f32x4){0.f, 0.f, 0.f, 0.f}; asm volatile("" : "+v"(acc[mi][n])); }
  SCHED();
  BAR();
  ST(1, 2);
#define PH(a_, b_)
  if (G == 1) BAR();
  SCHED();
  unsigned bo = G * G2_STG;
  for (int s = G; s < G2_NK; s += 2) {
    s16x8 a[4][2]; s16x4 l[2][4], h[2][4];
    unsigned pa[4], pw[4];
#pragma unroll
    for (int i = 0; i < 4; ++i) { pa[i] = ab[i] + bo; pw[i] = wb[i] + bo; }
#pragma unroll
    for (int i = 0; i < 4; ++i) { a[i][0] = rd128<0>(pa[i]); l[0][i] = tr_read<0>(pw[i]); h[0][i] = tr_read<1024>(pw[i]); }
#pragma unroll
    for (int i = 0; i < 4; ++i) { a[i][1] = rd128<1024>(pa[i]); l[1][i] = tr_read<8192>(pw[i]); h[1][i] = tr_read<9216>(pw[i]); }
    WAIT_L(12); BAR(); SCHED();
    __builtin_amdgcn_s_setprio(1);
#pragma unroll
    for (int kh = 0; kh < 2; ++kh) {
      if (kh) { SCHED(); WAIT_L0(); SCHED(); }
#pragma unroll
      for (int n = 0; n < 4; ++n) {
        const s16x8 w = PK8(l[kh][n], h[kh][n]);
#pragma unroll
        for (int mi = 0; mi < 4; ++mi) acc[mi][n] = MFMA(w, a[mi][kh], acc[mi][n]);
      }
    }
    __builtin_amdgcn_s_setprio(0);
    SCHED(); BAR(); SCHED();
    bo += 2 * G2_STG; if (bo >= G2_NST * G2_STG) bo -= G2_NST * G2_STG;
  }
  if (G == 0) BAR();
  SCHED();
  ST(1, 3);
  const int row = mt * 128 + wr * 64 + (2 * G + ks) * 16 + (lane & 15);
  const size_t ooff = (size_t)row * 2048 + (nt * 128 + wc * 64 + g * 8) * 2;
  const u32x4 xv0 = *(const u32x4*)(x + ooff), xv1 = *(const u32x4*)(x + ooff + 64);
#pragma unroll
  for (int i = 0; i < 2; ++i)
#pragma unroll
    for (int n = 0; n < 4; ++n) *(lf4*)(shm + ((wid * 2 + i) * 4 + n) * 1024 + lane16) = acc[2 + i][n];
  WAIT_L0(); BAR(); SCHED();
  f32x4 keep[4], send[4];
#pragma unroll
  for (int n = 0; n < 4; ++n) {
    send[n] = acc[1][n] + *(lf4*)(shm + (((wid ^ 4) * 2 + 1) * 4 + n) * 1024 + lane16);
    keep[n] = acc[0][n] + *(lf4*)(shm + (((wid ^ 4) * 2 + 0) * 4 + n) * 1024 + lane16);
  }
  const int tile = mt * 8 + nt;
  const rsrc_t rs = mkrs(exch + (size_t)tile * 65536, 65536);
#pragma unroll
  for (int n = 0; n < 4; ++n)
    __builtin_amdgcn_raw_buffer_store_b128(__builtin_bit_cast(u32x4, send[n]), rs, ks * 32768 + (wid * 4 + n) * 1024 + lane16, 0, 16);
  ST(1, 4);
  WAIT_V(0);
  ST(1, 5);
  if (lane == 0) {
    __hip_atomic_store((gu32*)(flags + ((tile * 2 + ks) * 8 + wid) * 32), 1u, __ATOMIC_RELAXED, __HIP_MEMORY_SCOPE_AGENT);
    while (__hip_atomic_load((gu32*)(flags + ((tile * 2 + (ks ^ 1)) * 8 + wid) * 32), __ATOMIC_RELAXED, __HIP_MEMORY_SCOPE_AGENT) == 0u) __builtin_amdgcn_s_sleep(1);
  }
  ST(1, 6);
  asm volatile("" ::: "memory");
#pragma unroll
  for (int n = 0; n < 4; ++n)
    keep[n] += __builtin_bit_cast(f32x4, __builtin_amdgcn_raw_buffer_load_b128(rs, (ks ^ 1) * 32768 + (wid * 4 + n) * 1024 + lane16, 0, 16));
  const bool odd = g & 1;
#pragma unroll
  for (int u = 0; u < 2; ++u) {
    f32x4 lo, hi;
#pragma unroll
    for (int e = 0; e < 4; ++e) { lo[e] = odd ? keep[2 * u + 1][e] : keep[2 * u][e]; hi[e] = odd ? keep[2 * u][e] : keep[2 * u + 1][e]; }
    const u32x4 xv = u ? xv1 : xv0;
    const u32x4 o = {pk2(lo[0] + lo16(xv[0]), lo[1] + hi16(xv[0])), pk2(lo[2] + lo16(xv[1]), lo[3] + hi16(xv[1])),
                     pk2(hi[0] + lo16(xv[2]), hi[1] + hi16(xv[2])), pk2(hi[2] + lo16(xv[3]), hi[3] + hi16(xv[3]))};
    *(u32x4*)(out + ooff + u * 64) = o;
  }
  ST(1, 8); ST(1, 7);
}

extern "C" void kernel_launch(void* const* d_in, const int* in_sizes, int n_in, void* d_out, int out_size,
                              void* d_ws, size_t ws_size, hipStream_t stream) {
  const char* x = (const char*)d_in[0];
  const char* wqkv = (const char*)d_in[1];
  const char* wo = (const char*)d_in[2];
  const char* w1 = (const char*)d_in[4];
  const char* w2 = (const char*)d_in[5];
  char* ws = (char*)d_ws;
  (void)hipFuncSetAttribute((const void*)gemm1_kernel, hipFuncAttributeMaxDynamicSharedMemorySize, G1_LDS);
  (void)hipFuncSetAttribute((const void*)gemm2_kernel, hipFuncAttributeMaxDynamicSharedMemorySize, G2_LDS);
  gemm1_kernel<<<256, 512, G1_LDS, stream>>>(x, wqkv, w1, ws + WS_ACAT, wo, w2, (unsigned*)(ws + WS_FLAG));
  gemm2_kernel<<<256, 768, G2_LDS, stream>>>(ws + WS_ACAT, wo, w2, x, (char*)d_out, ws + WS_EXCH, (unsigned*)(ws + WS_FLAG));
}
```

```cpp
#include <hip/hip_runtime.h>
#include <hip/hip_bf16.h>

typedef short s16x4 __attribute__((ext_vector_type(4)));
typedef short s16x8 __attribute__((ext_vector_type(8)));
typedef __bf16 bf16x2 __attribute__((ext_vector_type(2)));
typedef float f32x4 __attribute__((ext_vector_type(4)));
typedef unsigned u32x4 __attribute__((ext_vector_type(4)));
typedef unsigned u32x2 __attribute__((ext_vector_type(2)));
typedef __attribute__((address_space(3))) char lchar;
typedef __attribute__((address_space(3))) unsigned lu32;

template <int V> struct IC { static constexpr int value = V; };
#define BAR() __builtin_amdgcn_s_barrier()
#define SCHED() __builtin_amdgcn_sched_barrier(0)
#define WAIT_V(n) asm volatile("s_waitcnt vmcnt(%0)" ::"n"(n) : "memory")
#define WAIT_L(n) asm volatile("s_waitcnt lgkmcnt(%0)" ::"n"(n) : "memory")
#define WAIT_L0() asm volatile("s_waitcnt lgkmcnt(0)" ::: "memory")
#define PK8(L, H) (s16x8){L[0], L[1], L[2], L[3], H[0], H[1], H[2], H[3]}
#define MFMA(a, b, c) __builtin_amdgcn_mfma_f32_16x16x32_bf16(a, b, c, 0, 0, 0)

#define ST(k, i)
constexpr int SEQ = 2048, DM = 1024, DFF = 3584, KC = 4608, KB2 = KC / 32;
constexpr size_t WS_XN = 0, WS_ACAT = 4u << 20, WS_EXCH = 24u << 20, WS_FLAG = 40u << 20;

__device__ __forceinline__ unsigned pk2(float a, float b) {
  bf16x2 v = {(__bf16)a, (__bf16)b};
  return __builtin_bit_cast(unsigned, v);
}
__device__ __forceinline__ float rbf(float a) { return (float)(__bf16)a; }
__device__ __forceinline__ float lo16(unsigned u) { return __builtin_bit_cast(float, u << 16); }
__device__ __forceinline__ float hi16(unsigned u) { return __builtin_bit_cast(float, u & 0xffff0000u); }

typedef short s16x2 __attribute__((ext_vector_type(2)));
__device__ __forceinline__ unsigned relu2(unsigned u) {
  const s16x2 z = {0, 0};
  asm("" : "+v"(u));
  return __builtin_bit_cast(unsigned, __builtin_elementwise_max(__builtin_bit_cast(s16x2, u), z));
}
template <bool MAX> __device__ __forceinline__ float red16(float v) {
  const unsigned u = __builtin_bit_cast(unsigned, v);
  auto r = __builtin_amdgcn_permlane16_swap(u, u, false, false);
  const unsigned r0 = r[0], r1 = r[1];
  const float a = __builtin_bit_cast(float, r0), b = __builtin_bit_cast(float, r1);
  return MAX ? fmaxf(a, b) : a + b;
}
template <bool MAX> __device__ __forceinline__ float red32(float v) {
  const unsigned u = __builtin_bit_cast(unsigned, v);
  auto r = __builtin_amdgcn_permlane32_swap(u, u, false, false);
  const unsigned r0 = r[0], r1 = r[1];
  const float a = __builtin_bit_cast(float, r0), b = __builtin_bit_cast(float, r1);
  return MAX ? fmaxf(a, b) : a + b;
}
template <int OFF> __device__ __forceinline__ s16x4 tr_read(unsigned a) {
  s16x4 r; asm volatile("ds_read_b64_tr_b16 %0, %1 offset:%2" : "=&v"(r) : "v"(a), "i"(OFF) : "memory"); return r;
}
template <int OFF> __device__ __forceinline__ s16x8 rd128(unsigned a) {
  s16x8 r; asm volatile("ds_read_b128 %0, %1 offset:%2" : "=&v"(r) : "v"(a), "i"(OFF) : "memory"); return r;
}
typedef __amdgpu_buffer_rsrc_t rsrc_t;
__device__ __forceinline__ rsrc_t mkrs(const void* p, unsigned bytes) {
  return __builtin_amdgcn_make_buffer_rsrc((void*)p, 0, bytes, 0x00020000);
}
__device__ __forceinline__ const char* uni(const char* p) {
  const unsigned long long u = (unsigned long long)p;
  const unsigned lo = __builtin_amdgcn_readfirstlane((unsigned)u), hi = __builtin_amdgcn_readfirstlane((unsigned)(u >> 32));
  return (const char*)(((unsigned long long)hi << 32) | lo);
}
#define NX_ 4
#define NQ_ 3
#define NW_ 4
#define PFL 15
#define PFR 13
#define EPRIO 2
#define STPOL "sc1"
__device__ __forceinline__ void st128(char* p, u32x4 v) { asm volatile("global_store_dwordx4 %0, %1, off " STPOL "\n\ts_nop 1" :: "v"(p), "v"(v) : "memory"); }
__device__ __forceinline__ void st64(char* p, u32x2 v) { asm volatile("global_store_dwordx2 %0, %1, off " STPOL "\n\ts_nop 1" :: "v"(p), "v"(v) : "memory"); }
__device__ __forceinline__ void glds(const char* base, unsigned voff, unsigned soff, lchar* l) {
  asm volatile("s_mov_b32 m0, %2\n\ts_nop 0\n\tglobal_load_lds_dwordx4 %0, %1"
               :: "v"(voff), "s"(base + soff), "s"((unsigned)(size_t)l) : "memory");
}

constexpr int G1_XU = 34816, G1_WH = 3 * G1_XU, G1_WQ = G1_WH + 32768, G1_ONE = G1_WQ + 24576, G1_LDS = G1_ONE + 16, G1_NT = 16;
typedef __attribute__((address_space(3))) u32x4 lu4;
typedef __attribute__((address_space(3))) u32x2 lu2;
typedef __attribute__((address_space(3))) f32x4 lf4;
typedef float f32x2 __attribute__((ext_vector_type(2)));
typedef __attribute__((address_space(3))) f32x2 lf2;
typedef __attribute__((address_space(3))) float lf1;

#define MM "v_mfma_f32_16x16x32_bf16 "
#define AMMA(c, a, b) asm volatile(MM "%0, %1, %2, %0" : "+v"(c) : "v"(a), "v"(b))
#define MMA_DONE() asm volatile("s_nop 15\n\ts_nop 15" ::: "memory")
__device__ __forceinline__ float sel(unsigned m, float a, float b) {
  return __builtin_bit_cast(float, (__builtin_bit_cast(unsigned, a) & m) | (__builtin_bit_cast(unsigned, b) & ~m));
}
__device__ __forceinline__ float redg(float v) { return red32<false>(red16<false>(v)); }
__device__ __forceinline__ f32x2 ln_stat(float s, float q) {
  const float mean = s * (1.f / DM), var = fmaxf(q * (1.f / DM) - mean * mean, 0.f);
  return (f32x2){rbf(mean), __builtin_amdgcn_rcpf(rbf(__builtin_amdgcn_sqrtf(rbf(rbf(var) + 1e-5f))))};
}
__device__ __forceinline__ f32x4 nrm(f32x4 a, f32x4 cs, f32x2 st) { return (a - st[0] * cs) * st[1]; }

__global__ __launch_bounds__(512, 2) void gemm1_kernel(const char* __restrict__ x_, const char* __restrict__ wqkv_,
                                                       const char* __restrict__ w1_, char* __restrict__ acat,
                                                       const char* __restrict__ wo_, const char* __restrict__ w2_,
                                                       unsigned* __restrict__ flags) {
  extern __shared__ __attribute__((aligned(16))) char shm_[];
  lchar* shm = (lchar*)shm_;
  ST(0, 0); ST(0, 1);
  const int xcd = blockIdx.x & 7, idx = blockIdx.x >> 3;
  const int mt = (xcd & 1) * 4 + (idx & 3), j = (xcd >> 1) * 8 + (idx >> 2);
  const int wid = __builtin_amdgcn_readfirstlane(threadIdx.x >> 6), lane = threadIdx.x & 63;
  const int G = wid >> 2, R = wid & 3;
  const int g = lane >> 4, q = (lane >> 2) & 3, p = lane & 3, rr = lane & 15;
  const unsigned l0 = (unsigned)(size_t)shm, lane16 = lane * 16;
  const char* x = uni(x_);
#define ROT 0
  const int rot = ROT;
  auto tk = [&](int t) { return (t + rot) & (G1_NT - 1); };
  const unsigned xo0 = (lane >> 3) * 2048 + ((lane & 7) ^ g) * 16, xo1 = (lane >> 3) * 2048 + ((lane & 7) ^ (4 + g)) * 16;
  auto issueX = [&](int h, int t, int xbuf) {
#pragma unroll
    for (int a = 0; a < NX_; ++a)
      glds(x, (a & 1) ? xo1 : xo0, (mt * 256 + h * 128 + R * 32 + a * 8) * 2048 + tk(t) * 128, shm + xbuf + (h * 16 + R * 4 + a) * 1024);
  };
  const unsigned xr0 = l0 + (R * 64 + rr) * 128 + (g ^ (rr >> 1)) * 16, xr1 = xr0 ^ 64;
  f32x4 acc[4][7];
  const f32x4 z = {0.f, 0.f, 0.f, 0.f};
  f32x4 gs[2] = {z, z}, gq[2] = {z, z}, cs[2] = {z, z};
  s16x8 ones = {0x3f80, 0x3f80, 0x3f80, 0x3f80, 0x3f80, 0x3f80, 0x3f80, 0x3f80};
  asm volatile("" : "+v"(ones), "+v"(gs[0]), "+v"(gs[1]), "+v"(gq[0]), "+v"(gq[1]), "+v"(cs[0]), "+v"(cs[1]));
  lf2* stat = (lf2*)shm;
  lf1* csl = (lf1*)(shm + 2304);
  lf1* hst = (lf1*)(shm + 3584);
  auto diag = [&](f32x4 d) { const unsigned m1 = 0u - (rr & 1), m2 = 0u - ((rr >> 1) & 1); return sel(m2, sel(m1, d[3], d[2]), sel(m1, d[1], d[0])); };
  auto put_stats = [&]() {
    if (g == q) { stat[R * 64 + G * 32 + rr] = ln_stat(gs[0][0], diag(gq[0])); stat[R * 64 + G * 32 + 16 + rr] = ln_stat(gs[1][0], diag(gq[1])); }
  };
  const int fo = j & 1;
  const int mb0 = mt * 16 + R * 4;
  unsigned pfd = 0;

  if (G == 0) {
    const char* wq = uni(wqkv_);
    issueX(1, 0, 0);
    SCHED();
    unsigned wo[3];
#pragma unroll
    for (int k = 0; k < 3; ++k) {
      const int i = (3 * R + k) * 64 + lane, r = i / 12, ch = (i % 12) ^ ((r >> 3) & 1);
      wo[k] = r * 6144 + (ch >> 2) * 2048 + j * 64 + (ch & 3) * 16;
    }
    const int hrow = mt > 0 ? mt * 256 - 16 : 0;
    auto issueQ = [&](int t, int B) {
#pragma unroll
      for (int k = 0; k < NQ_; ++k) glds(wq, wo[k], tk(t) * 64 * 6144, shm + G1_WQ + B * 12288 + (3 * R + k) * 1024);
    };
    auto issueH = [&](int t, int xbuf) {
      if (R < 2) glds(x, R ? xo1 : xo0, (hrow + R * 8) * 2048 + tk(t) * 128, shm + xbuf + (32 + R) * 1024);
    };
    issueQ(0, 0); issueH(0, 0); issueH(1, G1_XU); issueX(1, 1, G1_XU);
    SCHED();
    if (threadIdx.x < 8) flags[(blockIdx.x * 8 + threadIdx.x) * 32] = 0u;
    unsigned wb[7];
#pragma unroll
    for (int n = 0; n < 7; ++n) {
      const int F = n < 6 ? ((n >> 1) + R) % 3 + fo : (fo ? 0 : 3), f = n < 6 ? (n & 1) : fo;
      wb[n] = l0 + G1_WH + ((8 * g + q) * 16 + 4 * (F ^ q) + (p ^ (g & 1))) * 16 + (((p & 1) ^ f) * 8);
    }
#pragma unroll
    for (int mi = 0; mi < 4; ++mi)
#pragma unroll
      for (int n = 0; n < 7; ++n) { acc[mi][n] = (f32x4){0.f, 0.f, 0.f, 0.f}; asm volatile("" : "+v"(acc[mi][n])); }
    SCHED();
    WAIT_V(7); BAR();
    ST(0, 2);
    SCHED();
    int xc = 0, xn2 = 2 * G1_XU;
    auto half = [&](auto Bc, auto Kc, int t) {
      constexpr int B = decltype(Bc)::value, KH = decltype(Kc)::value;
      s16x8 xa[4]; s16x4 l[7], h[7];
      const unsigned xp = (KH ? xr1 : xr0) + xc;
      xa[0] = rd128<0>(xp); xa[1] = rd128<2048>(xp); xa[2] = rd128<4096>(xp); xa[3] = rd128<6144>(xp);
#pragma unroll
      for (int n = 0; n < 7; ++n) { l[n] = tr_read<B * 16384 + KH * 8192>(wb[n]); h[n] = tr_read<B * 16384 + KH * 8192 + 1024>(wb[n]); }
      if (KH == 0) {
        if (t + 1 < G1_NT) { issueQ(t + 1, B ^ 1); if (t + 2 < G1_NT) issueH(t + 2, xn2); WAIT_V(7); } else WAIT_V(0);
      } else {
        if (t + 2 < G1_NT) { issueX(1, t + 2, xn2); WAIT_V(7); } else WAIT_V(3);
      }
      WAIT_L0(); BAR(); SCHED();
      __builtin_amdgcn_s_setprio(1);
      asm volatile("s_cmp_eq_u32 %6, 3\n\ts_cbranch_scc1 1f\n\t" MM "%0, %2, %5, %0\n\t" MM "%1, %3, %5, %1\n\ts_branch 2f\n"
                   "1:\n\t" MM "%0, %4, %5, %0\n2:"
                   : "+v"(cs[0]), "+v"(cs[1]) : "v"(PK8(l[0], h[0])), "v"(PK8(l[1], h[1])), "v"(PK8(l[6], h[6])), "v"(ones), "s"(R) : "scc");
      SCHED();
#pragma unroll
      for (int n = 0; n < 7; ++n) {
        const s16x8 w = PK8(l[n], h[n]);
#pragma unroll
        for (int mi = 0; mi < 4; ++mi) AMMA(acc[mi][n], w, xa[mi]);
      }
#pragma unroll
      for (int i = 0; i < 2; ++i) { AMMA(gs[i], ones, xa[i]); AMMA(gq[i], xa[i], xa[i]); }
      __builtin_amdgcn_s_setprio(0);
      SCHED(); BAR(); SCHED();
    };
    for (int t = 0; t < G1_NT; t += 2) {
      half(IC<0>{}, IC<0>{}, t); half(IC<0>{}, IC<1>{}, t);
      xn2 = xc; xc = xc == 2 * G1_XU ? 0 : xc + G1_XU;
      half(IC<1>{}, IC<0>{}, t + 1); half(IC<1>{}, IC<1>{}, t + 1);
      xn2 = xc; xc = xc == 2 * G1_XU ? 0 : xc + G1_XU;
    }
    BAR(); MMA_DONE(); SCHED();
    ST(0, 3);
    put_stats();
    if (rr == 0) { *(lf4*)(csl + 2 * R * 16 + 4 * g) = cs[0]; if (R < 3) *(lf4*)(csl + (2 * R + 1) * 16 + 4 * g) = cs[1]; }
    __syncthreads();
    f32x4 cv[7];
#pragma unroll
    for (int n = 0; n < 7; ++n) cv[n] = *(lf4*)(csl + (n < 6 ? 2 * (((n >> 1) + R) % 3) + (n & 1) : 6) * 16 + 4 * g);
    char* dst = acat + (size_t)((mb0 * KB2 + 32 + 7 * (j >> 1)) * 1024) + lane16;
#pragma unroll
    for (int mi = 0; mi < 4; ++mi) {
      const f32x2 st = stat[R * 64 + mi * 16 + rr];
#pragma unroll
      for (int u = 0; u < 3; ++u) {
        const f32x4 a = nrm(acc[mi][2 * u], cv[2 * u], st), b = nrm(acc[mi][2 * u + 1], cv[2 * u + 1], st);
        const u32x4 o = {relu2(pk2(a[0], a[1])), relu2(pk2(a[2], a[3])), relu2(pk2(b[0], b[1])), relu2(pk2(b[2], b[3]))};
        st128(dst + (mi * KB2 + 4 * fo + (u + R) % 3) * 1024, o);
      }
      const f32x4 a = nrm(acc[mi][6], cv[6], st);
      const u32x2 o = {relu2(pk2(a[0], a[1])), relu2(pk2(a[2], a[3]))};
      st64(dst + (mi * KB2 + 3) * 1024 + fo * 8, o);
    }
  } else {
    const char* w1 = uni(w1_);
    issueX(0, 0, 0);
    unsigned wo[4];
#pragma unroll
    for (int k = 0; k < 4; ++k) {
      const int r = R * 16 + k * 4 + g;
      wo[k] = r * 7168 + (j >> 1) * 448 + (j & 1) * 192 + (rr ^ (((r >> 3) & 1) | ((r & 3) << 2))) * 16;
    }
    auto issueW = [&](int t, int B) {
#pragma unroll
      for (int k = 0; k < NW_; ++k) glds(w1, wo[k], tk(t) * 64 * 7168, shm + G1_WH + B * 16384 + (R * 4 + k) * 1024);
    };
    issueW(0, 0); issueX(0, 1, G1_XU); issueW(1, 1);
    SCHED();
    const unsigned lb = l0 + G1_WQ + ((8 * g + q) * 12 + (p ^ (g & 1))) * 16;
    const unsigned wb0 = lb + (p & 1) * 8, wb1 = wb0 ^ 8, wbh = lb + (1 + (R >> 1)) * 64 + (((p & 1) ^ (R & 1)) * 8);
    const unsigned hra = l0 + 32768 + rr * 128 + (g ^ (rr >> 1)) * 16;
    unsigned hr0 = rr ? hra : l0 + G1_ONE, hr1 = rr ? hra ^ 64 : l0 + G1_ONE, hm1 = rr ? 1u : 0u;
    asm volatile("" : "+v"(hr0), "+v"(hr1), "+v"(hm1));
    if (threadIdx.x == 256) *(lu4*)(shm + G1_ONE) = __builtin_bit_cast(u32x4, ones);
    const unsigned wu = blockIdx.x * 4 + R;
    const char* tp; unsigned ts;
    {
      const unsigned n = wu * 17 + min(lane, 16), s = 2 + (n >= 8704u), r = n >= 8704u ? n - 8704u : n;
      tp = r < 3072u ? wqkv_ + s * 393216u + r * 128u : r < 6656u ? w1_ + s * 458752u + (r - 3072u) * 128u : x_ + (r - 6656u) * 2048u + s * 128u;
      ts = r < 3072u ? 786432u : r < 6656u ? 917504u : 256u;
    }
    auto touch = [&](int t) {
      if (t == 7) {
        const unsigned n = min(wu * 11 + min(lane, 10), 10535u);
        tp = n < 8192u ? w2_ + n * 128u : wo_ + (n - 8192u) * 128u;
        ts = n < 8192u ? 8192u * 128u : 2340u * 128u;
      }
      if (lane < 17) asm volatile("global_load_dword %0, %1, off" : "+v"(pfd) : "v"(tp) : "memory");
      tp += ts;
    };
#pragma unroll
    for (int mi = 0; mi < 4; ++mi)
#pragma unroll
      for (int n = 0; n < 6; ++n) { acc[mi][n] = (f32x4){0.f, 0.f, 0.f, 0.f}; asm volatile("" : "+v"(acc[mi][n])); }
    f32x4 hacc = z; asm volatile("" : "+v"(hacc));
    SCHED();
    WAIT_V(8); WAIT_L0(); BAR();
    ST(0, 2);
    BAR();
    SCHED();
    int xc = 0, xn2 = 2 * G1_XU;
    auto half = [&](auto Bc, auto Kc, int t) {
      constexpr int B = decltype(Bc)::value, KH = decltype(Kc)::value, WO = B * 12288 + KH * 6144;
      s16x8 xa[4], hx; s16x4 l[6], h[6], hl, hh;
      const unsigned xp = (KH ? xr1 : xr0) + xc;
      xa[0] = rd128<0>(xp); xa[1] = rd128<2048>(xp); xa[2] = rd128<4096>(xp); xa[3] = rd128<6144>(xp);
      l[0] = tr_read<WO>(wb0); h[0] = tr_read<WO + 768>(wb0); l[1] = tr_read<WO>(wb1); h[1] = tr_read<WO + 768>(wb1);
      l[2] = tr_read<WO + 64>(wb0); h[2] = tr_read<WO + 832>(wb0); l[3] = tr_read<WO + 64>(wb1); h[3] = tr_read<WO + 832>(wb1);
      l[4] = tr_read<WO + 128>(wb0); h[4] = tr_read<WO + 896>(wb0); l[5] = tr_read<WO + 128>(wb1); h[5] = tr_read<WO + 896>(wb1);
      hl = tr_read<WO>(wbh); hh = tr_read<WO + 768>(wbh);
      hx = rd128<0>(__umul24(hm1, xc) + (KH ? hr1 : hr0));
      if (KH == 0) {
        if (t + 2 < G1_NT) {
          issueX(0, t + 2, xn2);
          touch(t);
        }
      } else {
        if (t + 2 < G1_NT) { issueW(t + 2, B); WAIT_V(9); } else WAIT_V(0);
      }
      WAIT_L0(); BAR(); SCHED();
      __builtin_amdgcn_s_setprio(1);
      asm volatile("s_cmp_gt_u32 %5, 1\n\ts_cbranch_scc1 1f\n\ts_cmp_eq_u32 %5, 1\n\ts_cbranch_scc1 2f\n\t" MM "%0, %1, %4, %0\n\ts_branch 3f\n"
                   "1:\n\t" MM "%0, %3, %3, %0\n\ts_branch 3f\n2:\n\t" MM "%0, %2, %4, %0\n3:"
                   : "+v"(cs[0]) : "v"(PK8(l[0], h[0])), "v"(PK8(l[1], h[1])), "v"(hx), "v"(ones), "s"(R) : "scc");
      SCHED();
#pragma unroll
      for (int n = 0; n < 6; ++n) {
        const s16x8 w = PK8(l[n], h[n]);
#pragma unroll
        for (int mi = 0; mi < 4; ++mi) AMMA(acc[mi][n], w, xa[mi]);
      }
      AMMA(hacc, PK8(hl, hh), hx);
#pragma unroll
      for (int i = 0; i < 2; ++i) { AMMA(gs[i], ones, xa[2 + i]); AMMA(gq[i], xa[2 + i], xa[2 + i]); }
      __builtin_amdgcn_s_setprio(0);
      SCHED(); BAR(); SCHED();
    };
    for (int t = 0; t < G1_NT; t += 2) {
      half(IC<0>{}, IC<0>{}, t); half(IC<0>{}, IC<1>{}, t);
      xn2 = xc; xc = xc == 2 * G1_XU ? 0 : xc + G1_XU;
      half(IC<1>{}, IC<0>{}, t + 1); half(IC<1>{}, IC<1>{}, t + 1);
      xn2 = xc; xc = xc == 2 * G1_XU ? 0 : xc + G1_XU;
    }
    asm volatile("" :: "v"(pfd));
    MMA_DONE(); SCHED();
    __builtin_amdgcn_s_setprio(EPRIO);
    ST(0, 3);
    if (threadIdx.x == 256) *(lu32*)(shm + 3712) = 0u;
    put_stats();
    if (rr == 0) { *(lf4*)(csl + (9 + R) * 16 + 4 * g) = hacc; if (R < 2) *(lf4*)(csl + (7 + R) * 16 + 4 * g) = cs[0]; }
    if (R == 2) { if (g == 0) hst[rr] = cs[0][0]; if (g == q) hst[16 + rr] = diag(cs[0]); }
    __syncthreads();
    f32x4 cv[6];
#pragma unroll
    for (int n = 0; n < 6; ++n) cv[n] = *(lf4*)(csl + (7 + n) * 16 + 4 * g);
    u32x4 fr[4][3];
    f32x2 st[4];
#pragma unroll
    for (int mi = 0; mi < 4; ++mi) st[mi] = stat[R * 64 + mi * 16 + rr];
    auto mkfr = [&](int mi, int u) {
      const f32x4 a = nrm(acc[mi][2 * u], cv[2 * u], st[mi]), c = nrm(acc[mi][2 * u + 1], cv[2 * u + 1], st[mi]);
      fr[mi][u] = (u32x4){pk2(a[0], a[1]), pk2(a[2], a[3]), pk2(c[0], c[1]), pk2(c[2], c[3])};
    };
    mkfr(3, 1);
#pragma unroll
    for (int mi = 0; mi < 4; ++mi) mkfr(mi, 2);
    lchar* vimg = shm + 4096;
    lchar* kblk = shm + 21504;
#pragma unroll
    for (int mi = 0; mi < 4; ++mi) *(lu4*)(vimg + (16 + R * 64 + mi * 16 + rr) * 64 + g * 16) = fr[mi][2];
    if (R < 3) *(lu4*)(kblk + (R + 1) * 1024 + lane16) = fr[3][1];
    {
      const f32x4 a = nrm(hacc, *(lf4*)(csl + (9 + R) * 16 + 4 * g), ln_stat(hst[rr], hst[16 + rr]));
      lchar* d = ((R < 2) ? (kblk + lane16) : (vimg + rr * 64 + g * 16)) + (R & 1) * 8;
      *(lu2*)d = (u32x2){pk2(a[0], a[1]), pk2(a[2], a[3])};
    }
    WAIT_L0();
    if (lane == 0) __hip_atomic_fetch_add((lu32*)(shm + 3712), 1u, __ATOMIC_RELAXED, __HIP_MEMORY_SCOPE_WORKGROUP);
    SCHED();
#pragma unroll
    for (int mi = 0; mi < 4; ++mi) { mkfr(mi, 0); if (mi < 3) mkfr(mi, 1); }
    f32x4 s0[4], s1[4];
#pragma unroll
    for (int mi = 0; mi < 4; ++mi) {
      s1[mi] = MFMA(__builtin_bit_cast(s16x8, fr[mi][1]), __builtin_bit_cast(s16x8, fr[mi][0]), z);
      if (mi) s0[mi] = MFMA(__builtin_bit_cast(s16x8, fr[mi - 1][1]), __builtin_bit_cast(s16x8, fr[mi][0]), z);
    }
    SCHED();
    if (lane == 0) {
      while (__hip_atomic_load((lu32*)(shm + 3712), __ATOMIC_RELAXED, __HIP_MEMORY_SCOPE_WORKGROUP) < 4u) __builtin_amdgcn_s_sleep(1);
    }
    asm volatile("" ::: "memory");
    SCHED();
    const u32x4 kprev = *(lu4*)(kblk + R * 1024 + lane16);
    s0[0] = MFMA(__builtin_bit_cast(s16x8, kprev), __builtin_bit_cast(s16x8, fr[0][0]), z);
    const unsigned vb = l0 + 4096 + (R * 64 + 4 * g + q) * 64 + p * 16;
    s16x4 va[4], vc[4], vd[4], ve[4];
    va[0] = tr_read<0>(vb); vc[0] = tr_read<8>(vb); vd[0] = tr_read<1024>(vb); ve[0] = tr_read<1032>(vb);
    va[1] = tr_read<1024>(vb); vc[1] = tr_read<1032>(vb); vd[1] = tr_read<2048>(vb); ve[1] = tr_read<2056>(vb);
    va[2] = tr_read<2048>(vb); vc[2] = tr_read<2056>(vb); vd[2] = tr_read<3072>(vb); ve[2] = tr_read<3080>(vb);
    va[3] = tr_read<3072>(vb); vc[3] = tr_read<3080>(vb); vd[3] = tr_read<4096>(vb); ve[3] = tr_read<4104>(vb);
    const int k0 = 4 * g + (rr & 1);
    unsigned par = 0u - (unsigned)(rr & 1), c0 = (unsigned)((k0 - rr - 1) >> 31), c1 = (unsigned)((k0 + 1 - rr) >> 31);
    asm volatile("" : "+v"(par), "+v"(c0), "+v"(c1));
    const float scale = 0.17677669529663687f;
    float e0[4], e1[4], mx[4], sm[4];
#pragma unroll
    for (int mi = 0; mi < 4; ++mi) {
      const float a0 = sel(c0, sel(par, s1[mi][1], s1[mi][0]), sel(par, s0[mi][1], s0[mi][0]));
      const float a1 = sel(c1, sel(par, s1[mi][3], s1[mi][2]), sel(par, s0[mi][3], s0[mi][2]));
      e0[mi] = rbf(rbf(a0) * scale); e1[mi] = rbf(rbf(a1) * scale);
      if (mi == 0) { const unsigned ok = mb0 == 0 ? 0u : ~0u; e0[0] = sel(c0 | ok, e0[0], -1e9f); e1[0] = sel(c1 | ok, e1[0], -1e9f); }
      mx[mi] = fmaxf(e0[mi], e1[mi]);
    }
#pragma unroll
    for (int mi = 0; mi < 4; ++mi) mx[mi] = red16<true>(mx[mi]);
#pragma unroll
    for (int mi = 0; mi < 4; ++mi) mx[mi] = red32<true>(mx[mi]);
#pragma unroll
    for (int mi = 0; mi < 4; ++mi) { e0[mi] = __expf(e0[mi] - mx[mi]); e1[mi] = __expf(e1[mi] - mx[mi]); sm[mi] = e0[mi] + e1[mi]; }
#pragma unroll
    for (int mi = 0; mi < 4; ++mi) sm[mi] = red16<false>(sm[mi]);
#pragma unroll
    for (int mi = 0; mi < 4; ++mi) sm[mi] = red32<false>(sm[mi]);
    WAIT_L0(); SCHED();
    char* dst = acat + (size_t)((mb0 * KB2 + j) * 1024) + lane16;
#pragma unroll
    for (int mi = 0; mi < 4; ++mi) {
      const float inv = __builtin_amdgcn_rcpf(sm[mi]);
      const unsigned pr = pk2(e0[mi] * inv, e1[mi] * inv);
      const unsigned hm = par ^ 0xffffu;
      const unsigned p0 = ((pr << 16) | (pr & 0xffffu)) & hm, p1 = ((pr & 0xffff0000u) | (pr >> 16)) & hm;
      const u32x4 pp = {p0 & ~c0, p1 & ~c1, p0 & c0, p1 & c1};
      const s16x8 pf8 = __builtin_bit_cast(s16x8, pp);
      const f32x4 o0 = MFMA(PK8(va[mi], vd[mi]), pf8, z);
      const f32x4 o1 = MFMA(PK8(vc[mi], ve[mi]), pf8, z);
      const u32x4 o = {pk2(o0[0], o0[1]), pk2(o0[2], o0[3]), pk2(o1[0], o1[1]), pk2(o1[2], o1[3])};
      st128(dst + mi * KB2 * 1024, o);
    }
  }
  ST(0, 4);
  ST(0, 5); ST(0, 6); ST(0, 8); ST(0, 7);
}

#define G2NST 5
constexpr int G2_NST = G2NST, G2_STG = 32768, G2_LDS = G2_NST * G2_STG, G2_NK = 36, G2_PFD = G2_NST + 8;
typedef __attribute__((address_space(1))) unsigned gu32;

__global__ __launch_bounds__(768, 3) void gemm2_kernel(const char* __restrict__ acat, const char* __restrict__ wo_,
                                                       const char* __restrict__ w2_, const char* __restrict__ x,
                                                       char* __restrict__ out, char* __restrict__ exch,
                                                       unsigned* __restrict__ flags) {
  extern __shared__ __attribute__((aligned(16))) char shm_[];
  lchar* shm = (lchar*)shm_;
  ST(1, 0); ST(1, 1);
  const int xcd = blockIdx.x & 7, idx = blockIdx.x >> 3;
  const int ks = xcd & 1, mt = (xcd >> 1) * 4 + (idx & 3), nt = idx >> 2;
  const int wid = __builtin_amdgcn_readfirstlane(threadIdx.x >> 6), lane = threadIdx.x & 63;
  const int wl = wid & 3, G = wid >> 2;
  const unsigned lane16 = lane * 16;
  if (G == 2) {
    const char* A = uni(acat); const char* Wo = uni(wo_); const char* W2 = uni(w2_);
    const unsigned a0 = ((mt * 8 + 2 * wl) * KB2 + ks * 2 * G2_NK) * 1024;
    unsigned wo[4];
#pragma unroll
    for (int k = 0; k < 4; ++k) {
      const int r = wl * 16 + k * 4 + (lane >> 4), pos = lane & 15;
      wo[k] = (r ^ (((r >> 3) & 1) << 2)) * 2048 + (pos ^ (((r >> 3) & 1) | ((r & 3) << 2))) * 16 + nt * 256;
    }
    auto issue = [&](int s, int buf) {
      const int kt = ks * G2_NK + s;
      lchar* dst = shm + buf * G2_STG + wl * 4096;
      const char* Wr = kt < 16 ? Wo : W2;
      const unsigned so = (kt < 16 ? kt : kt - 16) * 131072;
#pragma unroll
      for (int k = 0; k < 4; ++k) {
        glds(A, lane16, a0 + s * 2048 + (k >> 1) * (KB2 * 1024) + (k & 1) * 1024, dst + k * 1024);
        glds(Wr, wo[k], so, dst + 16384 + k * 1024);
      }
    };
    issue(0, 0); issue(1, 1); issue(2, 2);
    WAIT_V(16); BAR();
    issue(3, 3);
    WAIT_V(16); BAR();
    int ib = 4;
    for (int j = 0; j < G2_NK; ++j) {
      if (j + 4 < G2_NK) { issue(j + 4, ib); WAIT_V(16); }
      else if (j + 3 < G2_NK) WAIT_V(8); else WAIT_V(0);
      BAR();
      ib = ib + 1 == G2_NST ? 0 : ib + 1;
    }
    BAR();
    return;
  }
  const int wr = wl >> 1, wc = wl & 1;
  const int g = lane >> 4, q = (lane >> 2) & 3, p = lane & 3;
  const unsigned l0 = (unsigned)(size_t)shm;
  unsigned ab[4], wb[4];
#pragma unroll
  for (int mi = 0; mi < 4; ++mi) ab[mi] = l0 + wr * 8192 + (mi ^ (2 * G + ks)) * 2048 + lane16;
#pragma unroll
  for (int n = 0; n < 4; ++n)
    wb[n] = l0 + 16384 + ((8 * g + q) * 16 + 4 * ((wc * 2 + (n >> 1)) ^ q) + (p ^ (g & 1))) * 16 + (((p & 1) ^ (n & 1)) * 8);
  f32x4 acc[4][4];
#pragma unroll
  for (int mi = 0; mi < 4; ++mi)
#pragma unroll
    for (int n = 0; n < 4; ++n) { acc[mi][n] = (f32x4){0.f, 0.f, 0.f, 0.f}; asm volatile("" : "+v"(acc[mi][n])); }
  SCHED();
  BAR();
  ST(1, 2);
#define PH(a_, b_)
  if (G == 1) BAR();
  SCHED();
  unsigned bo = G * G2_STG;
  for (int s = G; s < G2_NK; s += 2) {
    s16x8 a[4][2]; s16x4 l[2][4], h[2][4];
    unsigned pa[4], pw[4];
#pragma unroll
    for (int i = 0; i < 4; ++i) { pa[i] = ab[i] + bo; pw[i] = wb[i] + bo; }
#pragma unroll
    for (int i = 0; i < 4; ++i) { a[i][0] = rd128<0>(pa[i]); l[0][i] = tr_read<0>(pw[i]); h[0][i] = tr_read<1024>(pw[i]); }
#pragma unroll
    for (int i = 0; i < 4; ++i) { a[i][1] = rd128<1024>(pa[i]); l[1][i] = tr_read<8192>(pw[i]); h[1][i] = tr_read<9216>(pw[i]); }
    WAIT_L(12); BAR(); SCHED();
    __builtin_amdgcn_s_setprio(1);
#pragma unroll
    for (int kh = 0; kh < 2; ++kh) {
      if (kh) { SCHED(); WAIT_L0(); SCHED(); }
#pragma unroll
      for (int n = 0; n < 4; ++n) {
        const s16x8 w = PK8(l[kh][n], h[kh][n]);
#pragma unroll
        for (int mi = 0; mi < 4; ++mi) acc[mi][n] = MFMA(w, a[mi][kh], acc[mi][n]);
      }
    }
    __builtin_amdgcn_s_setprio(0);
    SCHED(); BAR(); SCHED();
    bo += 2 * G2_STG; if (bo >= G2_NST * G2_STG) bo -= G2_NST * G2_STG;
  }
  if (G == 0) BAR();
  SCHED();
  ST(1, 3);
  const int row = mt * 128 + wr * 64 + (2 * G + ks) * 16 + (lane & 15);
  const size_t ooff = (size_t)row * 2048 + (nt * 128 + wc * 64 + g * 8) * 2;
  const u32x4 xv0 = *(const u32x4*)(x + ooff), xv1 = *(const u32x4*)(x + ooff + 64);
#pragma unroll
  for (int i = 0; i < 2; ++i)
#pragma unroll
    for (int n = 0; n < 4; ++n) *(lf4*)(shm + ((wid * 2 + i) * 4 + n) * 1024 + lane16) = acc[2 + i][n];
  WAIT_L0(); BAR(); SCHED();
  f32x4 keep[4], send[4];
#pragma unroll
  for (int n = 0; n < 4; ++n) {
    send[n] = acc[1][n] + *(lf4*)(shm + (((wid ^ 4) * 2 + 1) * 4 + n) * 1024 + lane16);
    keep[n] = acc[0][n] + *(lf4*)(shm + (((wid ^ 4) * 2 + 0) * 4 + n) * 1024 + lane16);
  }
  const int tile = mt * 8 + nt;
  const rsrc_t rs = mkrs(exch + (size_t)tile * 65536, 65536);
#pragma unroll
  for (int n = 0; n < 4; ++n)
    __builtin_amdgcn_raw_buffer_store_b128(__builtin_bit_cast(u32x4, send[n]), rs, ks * 32768 + (wid * 4 + n) * 1024 + lane16, 0, 16);
  ST(1, 4);
  WAIT_V(0);
  ST(1, 5);
  if (lane == 0) {
    __hip_atomic_store((gu32*)(flags + ((tile * 2 + ks) * 8 + wid) * 32), 1u, __ATOMIC_RELAXED, __HIP_MEMORY_SCOPE_AGENT);
    while (__hip_atomic_load((gu32*)(flags + ((tile * 2 + (ks ^ 1)) * 8 + wid) * 32), __ATOMIC_RELAXED, __HIP_MEMORY_SCOPE_AGENT) == 0u) __builtin_amdgcn_s_sleep(1);
  }
  ST(1, 6);
  asm volatile("" ::: "memory");
#pragma unroll
  for (int n = 0; n < 4; ++n)
    keep[n] += __builtin_bit_cast(f32x4, __builtin_amdgcn_raw_buffer_load_b128(rs, (ks ^ 1) * 32768 + (wid * 4 + n) * 1024 + lane16, 0, 16));
  const bool odd = g & 1;
#pragma unroll
  for (int u = 0; u < 2; ++u) {
    f32x4 lo, hi;
#pragma unroll
    for (int e = 0; e < 4; ++e) { lo[e] = odd ? keep[2 * u + 1][e] : keep[2 * u][e]; hi[e] = odd ? keep[2 * u][e] : keep[2 * u + 1][e]; }
    const u32x4 xv = u ? xv1 : xv0;
    const u32x4 o = {pk2(lo[0] + lo16(xv[0]), lo[1] + hi16(xv[0])), pk2(lo[2] + lo16(xv[1]), lo[3] + hi16(xv[1])),
                     pk2(hi[0] + lo16(xv[2]), hi[1] + hi16(xv[2])), pk2(hi[2] + lo16(xv[3]), hi[3] + hi16(xv[3]))};
    *(u32x4*)(out + ooff + u * 64) = o;
  }
  ST(1, 8); ST(1, 7);
}

extern "C" void kernel_launch(void* const* d_in, const int* in_sizes, int n_in, void* d_out, int out_size,
                              void* d_ws, size_t ws_size, hipStream_t stream) {
  const char* x = (const char*)d_in[0];
  const char* wqkv = (const char*)d_in[1];
  const char* wo = (const char*)d_in[2];
  const char* w1 = (const char*)d_in[4];
  const char* w2 = (const char*)d_in[5];
  char* ws = (char*)d_ws;
  (void)hipFuncSetAttribute((const void*)gemm1_kernel, hipFuncAttributeMaxDynamicSharedMemorySize, G1_LDS);
  (void)hipFuncSetAttribute((const void*)gemm2_kernel, hipFuncAttributeMaxDynamicSharedMemorySize, G2_LDS);
  gemm1_kernel<<<256, 512, G1_LDS, stream>>>(x, wqkv, w1, ws + WS_ACAT, wo, w2, (unsigned*)(ws + WS_FLAG));
  gemm2_kernel<<<256, 768, G2_LDS, stream>>>(ws + WS_ACAT, wo, w2, x, (char*)d_out, ws + WS_EXCH, (unsigned*)(ws + WS_FLAG));
}
```

```cpp
#include <hip/hip_runtime.h>
#include <hip/hip_bf16.h>

typedef short s16x4 __attribute__((ext_vector_type(4)));
typedef short s16x8 __attribute__((ext_vector_type(8)));
typedef __bf16 bf16x2 __attribute__((ext_vector_type(2)));
typedef float f32x4 __attribute__((ext_vector_type(4)));
typedef unsigned u32x4 __attribute__((ext_vector_type(4)));
typedef unsigned u32x2 __attribute__((ext_vector_type(2)));
typedef __attribute__((address_space(3))) char lchar;
typedef __attribute__((address_space(3))) unsigned lu32;

template <int V> struct IC { static constexpr int value = V; };
#define BAR() __builtin_amdgcn_s_barrier()
#define SCHED() __builtin_amdgcn_sched_barrier(0)
#define WAIT_V(n) asm volatile("s_waitcnt vmcnt(%0)" ::"n"(n) : "memory")
#define WAIT_L(n) asm volatile("s_waitcnt lgkmcnt(%0)" ::"n"(n) : "memory")
#define WAIT_L0() asm volatile("s_waitcnt lgkmcnt(0)" ::: "memory")
#define PK8(L, H) (s16x8){L[0], L[1], L[2], L[3], H[0], H[1], H[2], H[3]}
#define MFMA(a, b, c) __builtin_amdgcn_mfma_f32_16x16x32_bf16(a, b, c, 0, 0, 0)

#define ST(k, i)
constexpr int SEQ = 2048, DM = 1024, DFF = 3584, KC = 4608, KB2 = KC / 32;
constexpr size_t WS_XN = 0, WS_ACAT = 4u << 20, WS_EXCH = 24u << 20, WS_FLAG = 40u << 20;

__device__ __forceinline__ unsigned pk2(float a, float b) {
  bf16x2 v = {(__bf16)a, (__bf16)b};
  return __builtin_bit_cast(unsigned, v);
}
__device__ __forceinline__ float rbf(float a) { return (float)(__bf16)a; }
__device__ __forceinline__ float lo16(unsigned u) { return __builtin_bit_cast(float, u << 16); }
__device__ __forceinline__ float hi16(unsigned u) { return __builtin_bit_cast(float, u & 0xffff0000u); }

typedef short s16x2 __attribute__((ext_vector_type(2)));
__device__ __forceinline__ unsigned relu2(unsigned u) {
  const s16x2 z = {0, 0};
  asm("" : "+v"(u));
  return __builtin_bit_cast(unsigned, __builtin_elementwise_max(__builtin_bit_cast(s16x2, u), z));
}
template <bool MAX> __device__ __forceinline__ float red16(float v) {
  const unsigned u = __builtin_bit_cast(unsigned, v);
  auto r = __builtin_amdgcn_permlane16_swap(u, u, false, false);
  const unsigned r0 = r[0], r1 = r[1];
  const float a = __builtin_bit_cast(float, r0), b = __builtin_bit_cast(float, r1);
  return MAX ? fmaxf(a, b) : a + b;
}
template <bool MAX> __device__ __forceinline__ float red32(float v) {
  const unsigned u = __builtin_bit_cast(unsigned, v);
  auto r = __builtin_amdgcn_permlane32_swap(u, u, false, false);
  const unsigned r0 = r[0], r1 = r[1];
  const float a = __builtin_bit_cast(float, r0), b = __builtin_bit_cast(float, r1);
  return MAX ? fmaxf(a, b) : a + b;
}
template <int OFF> __device__ __forceinline__ s16x4 tr_read(unsigned a) {
  s16x4 r; asm volatile("ds_read_b64_tr_b16 %0, %1 offset:%2" : "=&v"(r) : "v"(a), "i"(OFF) : "memory"); return r;
}
template <int OFF> __device__ __forceinline__ s16x8 rd128(unsigned a) {
  s16x8 r; asm volatile("ds_read_b128 %0, %1 offset:%2" : "=&v"(r) : "v"(a), "i"(OFF) : "memory"); return r;
}
typedef __amdgpu_buffer_rsrc_t rsrc_t;
__device__ __forceinline__ rsrc_t mkrs(const void* p, unsigned bytes) {
  return __builtin_amdgcn_make_buffer_rsrc((void*)p, 0, bytes, 0x00020000);
}
__device__ __forceinline__ const char* uni(const char* p) {
  const unsigned long long u = (unsigned long long)p;
  const unsigned lo = __builtin_amdgcn_readfirstlane((unsigned)u), hi = __builtin_amdgcn_readfirstlane((unsigned)(u >> 32));
  return (const char*)(((unsigned long long)hi << 32) | lo);
}
#define NX_ 4
#define NQ_ 3
#define NW_ 4
#define PFL 15
#define PFR 13
#define EPRIO 0
#define STPOL "sc1"
__device__ __forceinline__ void st128(char* p, u32x4 v) { asm volatile("global_store_dwordx4 %0, %1, off " STPOL "\n\ts_nop 1" :: "v"(p), "v"(v) : "memory"); }
__device__ __forceinline__ void st64(char* p, u32x2 v) { asm volatile("global_store_dwordx2 %0, %1, off " STPOL "\n\ts_nop 1" :: "v"(p), "v"(v) : "memory"); }
__device__ __forceinline__ void glds(const char* base, unsigned voff, unsigned soff, lchar* l) {
  asm volatile("s_mov_b32 m0, %2\n\ts_nop 0\n\tglobal_load_lds_dwordx4 %0, %1"
               :: "v"(voff), "s"(base + soff), "s"((unsigned)(size_t)l) : "memory");
}

constexpr int G1_XU = 34816, G1_WH = 3 * G1_XU, G1_WQ = G1_WH + 32768, G1_ONE = G1_WQ + 24576, G1_LDS = G1_ONE + 16, G1_NT = 16;
typedef __attribute__((address_space(3))) u32x4 lu4;
typedef __attribute__((address_space(3))) u32x2 lu2;
typedef __attribute__((address_space(3))) f32x4 lf4;
typedef float f32x2 __attribute__((ext_vector_type(2)));
typedef __attribute__((address_space(3))) f32x2 lf2;
typedef __attribute__((address_space(3))) float lf1;

#define MM "v_mfma_f32_16x16x32_bf16 "
#define AMMA(c, a, b) asm volatile(MM "%0, %1, %2, %0" : "+v"(c) : "v"(a), "v"(b))
#define MMA_DONE() asm volatile("s_nop 15\n\ts_nop 15" ::: "memory")
__device__ __forceinline__ float sel(unsigned m, float a, float b) {
  return __builtin_bit_cast(float, (__builtin_bit_cast(unsigned, a) & m) | (__builtin_bit_cast(unsigned, b) & ~m));
}
__device__ __forceinline__ float redg(float v) { return red32<false>(red16<false>(v)); }
__device__ __forceinline__ f32x2 ln_stat(float s, float q) {
  const float mean = s * (1.f / DM), var = fmaxf(q * (1.f / DM) - mean * mean, 0.f);
  return (f32x2){rbf(mean), __builtin_amdgcn_rcpf(rbf(__builtin_amdgcn_sqrtf(rbf(rbf(var) + 1e-5f))))};
}
__device__ __forceinline__ f32x4 nrm(f32x4 a, f32x4 cs, f32x2 st) { return (a - st[0] * cs) * st[1]; }

__global__ __launch_bounds__(512, 2) void gemm1_kernel(const char* __restrict__ x_, const char* __restrict__ wqkv_,
                                                       const char* __restrict__ w1_, char* __restrict__ acat,
                                                       const char* __restrict__ wo_, const char* __restrict__ w2_,
                                                       unsigned* __restrict__ flags) {
  extern __shared__ __attribute__((aligned(16))) char shm_[];
  lchar* shm = (lchar*)shm_;
  ST(0, 0); ST(0, 1);
  const int xcd = blockIdx.x & 7, idx = blockIdx.x >> 3;
  const int mt = (xcd & 1) * 4 + (idx & 3), j = (xcd >> 1) * 8 + (idx >> 2);
  const int wid = __builtin_amdgcn_readfirstlane(threadIdx.x >> 6), lane = threadIdx.x & 63;
  const int G = wid >> 2, R = wid & 3;
  const int g = lane >> 4, q = (lane >> 2) & 3, p = lane & 3, rr = lane & 15;
  const unsigned l0 = (unsigned)(size_t)shm, lane16 = lane * 16;
  const char* x = uni(x_);
#define ROT 0
  const int rot = ROT;
  auto tk = [&](int t) { return (t + rot) & (G1_NT - 1); };
  const unsigned xo0 = (lane >> 3) * 2048 + ((lane & 7) ^ g) * 16, xo1 = (lane >> 3) * 2048 + ((lane & 7) ^ (4 + g)) * 16;
  auto issueX = [&](int h, int t, int xbuf) {
#pragma unroll
    for (int a = 0; a < NX_; ++a)
      glds(x, (a & 1) ? xo1 : xo0, (mt * 256 + h * 128 + R * 32 + a * 8) * 2048 + tk(t) * 128, shm + xbuf + (h * 16 + R * 4 + a) * 1024);
  };
  const unsigned xr0 = l0 + (R * 64 + rr) * 128 + (g ^ (rr >> 1)) * 16, xr1 = xr0 ^ 64;
  f32x4 acc[4][7];
  const f32x4 z = {0.f, 0.f, 0.f, 0.f};
  f32x4 gs[2] = {z, z}, gq[2] = {z, z}, cs[2] = {z, z};
  s16x8 ones = {0x3f80, 0x3f80, 0x3f80, 0x3f80, 0x3f80, 0x3f80, 0x3f80, 0x3f80};
  asm volatile("" : "+v"(ones), "+v"(gs[0]), "+v"(gs[1]), "+v"(gq[0]), "+v"(gq[1]), "+v"(cs[0]), "+v"(cs[1]));
  lf2* stat = (lf2*)shm;
  lf1* csl = (lf1*)(shm + 2304);
  lf1* hst = (lf1*)(shm + 3584);
  auto diag = [&](f32x4 d) { const unsigned m1 = 0u - (rr & 1), m2 = 0u - ((rr >> 1) & 1); return sel(m2, sel(m1, d[3], d[2]), sel(m1, d[1], d[0])); };
  auto put_stats = [&]() {
    if (g == q) { stat[R * 64 + G * 32 + rr] = ln_stat(gs[0][0], diag(gq[0])); stat[R * 64 + G * 32 + 16 + rr] = ln_stat(gs[1][0], diag(gq[1])); }
  };
  const int fo = j & 1;
  const int mb0 = mt * 16 + R * 4;
  unsigned pfd = 0;

  if (G == 0) {
    const char* wq = uni(wqkv_);
    issueX(1, 0, 0);
    SCHED();
    unsigned wo[3];
#pragma unroll
    for (int k = 0; k < 3; ++k) {
      const int i = (3 * R + k) * 64 + lane, r = i / 12, ch = (i % 12) ^ ((r >> 3) & 1);
      wo[k] = r * 6144 + (ch >> 2) * 2048 + j * 64 + (ch & 3) * 16;
    }
    const int hrow = mt > 0 ? mt * 256 - 16 : 0;
    auto issueQ = [&](int t, int B) {
#pragma unroll
      for (int k = 0; k < NQ_; ++k) glds(wq, wo[k], tk(t) * 64 * 6144, shm + G1_WQ + B * 12288 + (3 * R + k) * 1024);
    };
    auto issueH = [&](int t, int xbuf) {
      if (R < 2) glds(x, R ? xo1 : xo0, (hrow + R * 8) * 2048 + tk(t) * 128, shm + xbuf + (32 + R) * 1024);
    };
    issueQ(0, 0); issueH(0, 0); issueH(1, G1_XU); issueX(1, 1, G1_XU);
    SCHED();
    if (threadIdx.x < 8) flags[(blockIdx.x * 8 + threadIdx.x) * 32] = 0u;
    unsigned wb[7];
#pragma unroll
    for (int n = 0; n < 7; ++n) {
      const int F = n < 6 ? ((n >> 1) + R) % 3 + fo : (fo ? 0 : 3), f = n < 6 ? (n & 1) : fo;
      wb[n] = l0 + G1_WH + ((8 * g + q) * 16 + 4 * (F ^ q) + (p ^ (g & 1))) * 16 + (((p & 1) ^ f) * 8);
    }
#pragma unroll
    for (int mi = 0; mi < 4; ++mi)
#pragma unroll
      for (int n = 0; n < 7; ++n) { acc[mi][n] = (f32x4){0.f, 0.f, 0.f, 0.f}; asm volatile("" : "+v"(acc[mi][n])); }
    SCHED();
    WAIT_V(7); BAR();
    ST(0, 2);
    SCHED();
    int xc = 0, xn2 = 2 * G1_XU;
    auto half = [&](auto Bc, auto Kc, int t) {
      constexpr int B = decltype(Bc)::value, KH = decltype(Kc)::value;
      s16x8 xa[4]; s16x4 l[7], h[7];
      const unsigned xp = (KH ? xr1 : xr0) + xc;
      xa[0] = rd128<0>(xp); xa[1] = rd128<2048>(xp); xa[2] = rd128<4096>(xp); xa[3] = rd128<6144>(xp);
#pragma unroll
      for (int n = 0; n < 7; ++n) { l[n] = tr_read<B * 16384 + KH * 8192>(wb[n]); h[n] = tr_read<B * 16384 + KH * 8192 + 1024>(wb[n]); }
      if (KH == 0) {
        if (t + 1 < G1_NT) { issueQ(t + 1, B ^ 1); if (t + 2 < G1_NT) issueH(t + 2, xn2); WAIT_V(7); } else WAIT_V(0);
      } else {
        if (t + 2 < G1_NT) { issueX(1, t + 2, xn2); WAIT_V(7); } else WAIT_V(3);
      }
      WAIT_L0(); BAR(); SCHED();
      __builtin_amdgcn_s_setprio(1);
      asm volatile("s_cmp_eq_u32 %6, 3\n\ts_cbranch_scc1 1f\n\t" MM "%0, %2, %5, %0\n\t" MM "%1, %3, %5, %1\n\ts_branch 2f\n"
                   "1:\n\t" MM "%0, %4, %5, %0\n2:"
                   : "+v"(cs[0]), "+v"(cs[1]) : "v"(PK8(l[0], h[0])), "v"(PK8(l[1], h[1])), "v"(PK8(l[6], h[6])), "v"(ones), "s"(R) : "scc");
      SCHED();
#pragma unroll
      for (int n = 0; n < 7; ++n) {
        const s16x8 w = PK8(l[n], h[n]);
#pragma unroll
        for (int mi = 0; mi < 4; ++mi) AMMA(acc[mi][n], w, xa[mi]);
      }
#pragma unroll
      for (int i = 0; i < 2; ++i) { AMMA(gs[i], ones, xa[i]); AMMA(gq[i], xa[i], xa[i]); }
      __builtin_amdgcn_s_setprio(0);
      SCHED(); BAR(); SCHED();
    };
    for (int t = 0; t < G1_NT; t += 2) {
      half(IC<0>{}, IC<0>{}, t); half(IC<0>{}, IC<1>{}, t);
      xn2 = xc; xc = xc == 2 * G1_XU ? 0 : xc + G1_XU;
      half(IC<1>{}, IC<0>{}, t + 1); half(IC<1>{}, IC<1>{}, t + 1);
      xn2 = xc; xc = xc == 2 * G1_XU ? 0 : xc + G1_XU;
    }
    BAR(); MMA_DONE(); SCHED();
    ST(0, 3);
    put_stats();
    if (rr == 0) { *(lf4*)(csl + 2 * R * 16 + 4 * g) = cs[0]; if (R < 3) *(lf4*)(csl + (2 * R + 1) * 16 + 4 * g) = cs[1]; }
    __syncthreads();
    f32x4 cv[7];
#pragma unroll
    for (int n = 0; n < 7; ++n) cv[n] = *(lf4*)(csl + (n < 6 ? 2 * (((n >> 1) + R) % 3) + (n & 1) : 6) * 16 + 4 * g);
    char* dst = acat + (size_t)((mb0 * KB2 + 32 + 7 * (j >> 1)) * 1024) + lane16;
#pragma unroll
    for (int mi = 0; mi < 4; ++mi) {
      const f32x2 st = stat[R * 64 + mi * 16 + rr];
#pragma unroll
      for (int u = 0; u < 3; ++u) {
        const f32x4 a = nrm(acc[mi][2 * u], cv[2 * u], st), b = nrm(acc[mi][2 * u + 1], cv[2 * u + 1], st);
        const u32x4 o = {relu2(pk2(a[0], a[1])), relu2(pk2(a[2], a[3])), relu2(pk2(b[0], b[1])), relu2(pk2(b[2], b[3]))};
        st128(dst + (mi * KB2 + 4 * fo + (u + R) % 3) * 1024, o);
      }
      const f32x4 a = nrm(acc[mi][6], cv[6], st);
      const u32x2 o = {relu2(pk2(a[0], a[1])), relu2(pk2(a[2], a[3]))};
      st64(dst + (mi * KB2 + 3) * 1024 + fo * 8, o);
    }
  } else {
    const char* w1 = uni(w1_);
    issueX(0, 0, 0);
    unsigned wo[4];
#pragma unroll
    for (int k = 0; k < 4; ++k) {
      const int r = R * 16 + k * 4 + g;
      wo[k] = r * 7168 + (j >> 1) * 448 + (j & 1) * 192 + (rr ^ (((r >> 3) & 1) | ((r & 3) << 2))) * 16;
    }
    auto issueW = [&](int t, int B) {
#pragma unroll
      for (int k = 0; k < NW_; ++k) glds(w1, wo[k], tk(t) * 64 * 7168, shm + G1_WH + B * 16384 + (R * 4 + k) * 1024);
    };
    issueW(0, 0); issueX(0, 1, G1_XU); issueW(1, 1);
    SCHED();
    const unsigned lb = l0 + G1_WQ + ((8 * g + q) * 12 + (p ^ (g & 1))) * 16;
    const unsigned wb0 = lb + (p & 1) * 8, wb1 = wb0 ^ 8, wbh = lb + (1 + (R >> 1)) * 64 + (((p & 1) ^ (R & 1)) * 8);
    const unsigned hra = l0 + 32768 + rr * 128 + (g ^ (rr >> 1)) * 16;
    unsigned hr0 = rr ? hra : l0 + G1_ONE, hr1 = rr ? hra ^ 64 : l0 + G1_ONE, hm1 = rr ? 1u : 0u;
    asm volatile("" : "+v"(hr0), "+v"(hr1), "+v"(hm1));
    if (threadIdx.x == 256) *(lu4*)(shm + G1_ONE) = __builtin_bit_cast(u32x4, ones);
    const unsigned wu = blockIdx.x * 4 + R;
    const char* tp; unsigned ts;
    {
      const unsigned n = wu * 17 + min(lane, 16), s = 2 + (n >= 8704u), r = n >= 8704u ? n - 8704u : n;
      tp = r < 3072u ? wqkv_ + s * 393216u + r * 128u : r < 6656u ? w1_ + s * 458752u + (r - 3072u) * 128u : x_ + (r - 6656u) * 2048u + s * 128u;
      ts = r < 3072u ? 786432u : r < 6656u ? 917504u : 256u;
    }
    auto touch = [&](int t) {
      if (t == 7) {
        const unsigned n = min(wu * 11 + min(lane, 10), 10535u);
        tp = n < 8192u ? w2_ + n * 128u : wo_ + (n - 8192u) * 128u;
        ts = n < 8192u ? 8192u * 128u : 2340u * 128u;
        if (lane > 10) {
          tp = acat + (size_t)(((mb0 + ((lane - 11) & 3)) * KB2 + (lane < 15 ? j : 32 + 7 * (j >> 1) + 4 * fo)) * 1024);
          ts = 0;
        }
      }
      if (lane < 19) asm volatile("global_load_dword %0, %1, off" : "+v"(pfd) : "v"(tp) : "memory");
      tp += ts;
    };
#pragma unroll
    for (int mi = 0; mi < 4; ++mi)
#pragma unroll
      for (int n = 0; n < 6; ++n) { acc[mi][n] = (f32x4){0.f, 0.f, 0.f, 0.f}; asm volatile("" : "+v"(acc[mi][n])); }
    f32x4 hacc = z; asm volatile("" : "+v"(hacc));
    SCHED();
    WAIT_V(8); WAIT_L0(); BAR();
    ST(0, 2);
    BAR();
    SCHED();
    int xc = 0, xn2 = 2 * G1_XU;
    auto half = [&](auto Bc, auto Kc, int t) {
      constexpr int B = decltype(Bc)::value, KH = decltype(Kc)::value, WO = B * 12288 + KH * 6144;
      s16x8 xa[4], hx; s16x4 l[6], h[6], hl, hh;
      const unsigned xp = (KH ? xr1 : xr0) + xc;
      xa[0] = rd128<0>(xp); xa[1] = rd128<2048>(xp); xa[2] = rd128<4096>(xp); xa[3] = rd128<6144>(xp);
      l[0] = tr_read<WO>(wb0); h[0] = tr_read<WO + 768>(wb0); l[1] = tr_read<WO>(wb1); h[1] = tr_read<WO + 768>(wb1);
      l[2] = tr_read<WO + 64>(wb0); h[2] = tr_read<WO + 832>(wb0); l[3] = tr_read<WO + 64>(wb1); h[3] = tr_read<WO + 832>(wb1);
      l[4] = tr_read<WO + 128>(wb0); h[4] = tr_read<WO + 896>(wb0); l[5] = tr_read<WO + 128>(wb1); h[5] = tr_read<WO + 896>(wb1);
      hl = tr_read<WO>(wbh); hh = tr_read<WO + 768>(wbh);
      hx = rd128<0>(__umul24(hm1, xc) + (KH ? hr1 : hr0));
      if (KH == 0) {
        if (t + 2 < G1_NT) {
          issueX(0, t + 2, xn2);
          touch(t);
        }
      } else {
        if (t + 2 < G1_NT) { issueW(t + 2, B); WAIT_V(9); } else WAIT_V(0);
      }
      WAIT_L0(); BAR(); SCHED();
      __builtin_amdgcn_s_setprio(1);
      asm volatile("s_cmp_gt_u32 %5, 1\n\ts_cbranch_scc1 1f\n\ts_cmp_eq_u32 %5, 1\n\ts_cbranch_scc1 2f\n\t" MM "%0, %1, %4, %0\n\ts_branch 3f\n"
                   "1:\n\t" MM "%0, %3, %3, %0\n\ts_branch 3f\n2:\n\t" MM "%0, %2, %4, %0\n3:"
                   : "+v"(cs[0]) : "v"(PK8(l[0], h[0])), "v"(PK8(l[1], h[1])), "v"(hx), "v"(ones), "s"(R) : "scc");
      SCHED();
#pragma unroll
      for (int n = 0; n < 6; ++n) {
        const s16x8 w = PK8(l[n], h[n]);
#pragma unroll
        for (int mi = 0; mi < 4; ++mi) AMMA(acc[mi][n], w, xa[mi]);
      }
      AMMA(hacc, PK8(hl, hh), hx);
#pragma unroll
      for (int i = 0; i < 2; ++i) { AMMA(gs[i], ones, xa[2 + i]); AMMA(gq[i], xa[2 + i], xa[2 + i]); }
      __builtin_amdgcn_s_setprio(0);
      SCHED(); BAR(); SCHED();
    };
    for (int t = 0; t < G1_NT; t += 2) {
      half(IC<0>{}, IC<0>{}, t); half(IC<0>{}, IC<1>{}, t);
      xn2 = xc; xc = xc == 2 * G1_XU ? 0 : xc + G1_XU;
      half(IC<1>{}, IC<0>{}, t + 1); half(IC<1>{}, IC<1>{}, t + 1);
      xn2 = xc; xc = xc == 2 * G1_XU ? 0 : xc + G1_XU;
    }
    asm volatile("" :: "v"(pfd));
    MMA_DONE(); SCHED();
    __builtin_amdgcn_s_setprio(EPRIO);
    ST(0, 3);
    if (threadIdx.x == 256) *(lu32*)(shm + 3712) = 0u;
    put_stats();
    if (rr == 0) { *(lf4*)(csl + (9 + R) * 16 + 4 * g) = hacc; if (R < 2) *(lf4*)(csl + (7 + R) * 16 + 4 * g) = cs[0]; }
    if (R == 2) { if (g == 0) hst[rr] = cs[0][0]; if (g == q) hst[16 + rr] = diag(cs[0]); }
    __syncthreads();
    f32x4 cv[6];
#pragma unroll
    for (int n = 0; n < 6; ++n) cv[n] = *(lf4*)(csl + (7 + n) * 16 + 4 * g);
    u32x4 fr[4][3];
    f32x2 st[4];
#pragma unroll
    for (int mi = 0; mi < 4; ++mi) st[mi] = stat[R * 64 + mi * 16 + rr];
    auto mkfr = [&](int mi, int u) {
      const f32x4 a = nrm(acc[mi][2 * u], cv[2 * u], st[mi]), c = nrm(acc[mi][2 * u + 1], cv[2 * u + 1], st[mi]);
      fr[mi][u] = (u32x4){pk2(a[0], a[1]), pk2(a[2], a[3]), pk2(c[0], c[1]), pk2(c[2], c[3])};
    };
    mkfr(3, 1);
#pragma unroll
    for (int mi = 0; mi < 4; ++mi) mkfr(mi, 2);
    lchar* vimg = shm + 4096;
    lchar* kblk = shm + 21504;
#pragma unroll
    for (int mi = 0; mi < 4; ++mi) *(lu4*)(vimg + (16 + R * 64 + mi * 16 + rr) * 64 + g * 16) = fr[mi][2];
    if (R < 3) *(lu4*)(kblk + (R + 1) * 1024 + lane16) = fr[3][1];
    {
      const f32x4 a = nrm(hacc, *(lf4*)(csl + (9 + R) * 16 + 4 * g), ln_stat(hst[rr], hst[16 + rr]));
      lchar* d = ((R < 2) ? (kblk + lane16) : (vimg + rr * 64 + g * 16)) + (R & 1) * 8;
      *(lu2*)d = (u32x2){pk2(a[0], a[1]), pk2(a[2], a[3])};
    }
    WAIT_L0();
    if (lane == 0) __hip_atomic_fetch_add((lu32*)(shm + 3712), 1u, __ATOMIC_RELAXED, __HIP_MEMORY_SCOPE_WORKGROUP);
    SCHED();
#pragma unroll
    for (int mi = 0; mi < 4; ++mi) { mkfr(mi, 0); if (mi < 3) mkfr(mi, 1); }
    f32x4 s0[4], s1[4];
#pragma unroll
    for (int mi = 0; mi < 4; ++mi) {
      s1[mi] = MFMA(__builtin_bit_cast(s16x8, fr[mi][1]), __builtin_bit_cast(s16x8, fr[mi][0]), z);
      if (mi) s0[mi] = MFMA(__builtin_bit_cast(s16x8, fr[mi - 1][1]), __builtin_bit_cast(s16x8, fr[mi][0]), z);
    }
    SCHED();
    if (lane == 0) {
      while (__hip_atomic_load((lu32*)(shm + 3712), __ATOMIC_RELAXED, __HIP_MEMORY_SCOPE_WORKGROUP) < 4u) __builtin_amdgcn_s_sleep(1);
    }
    asm volatile("" ::: "memory");
    SCHED();
    const u32x4 kprev = *(lu4*)(kblk + R * 1024 + lane16);
    s0[0] = MFMA(__builtin_bit_cast(s16x8, kprev), __builtin_bit_cast(s16x8, fr[0][0]), z);
    const unsigned vb = l0 + 4096 + (R * 64 + 4 * g + q) * 64 + p * 16;
    s16x4 va[4], vc[4], vd[4], ve[4];
    va[0] = tr_read<0>(vb); vc[0] = tr_read<8>(vb); vd[0] = tr_read<1024>(vb); ve[0] = tr_read<1032>(vb);
    va[1] = tr_read<1024>(vb); vc[1] = tr_read<1032>(vb); vd[1] = tr_read<2048>(vb); ve[1] = tr_read<2056>(vb);
    va[2] = tr_read<2048>(vb); vc[2] = tr_read<2056>(vb); vd[2] = tr_read<3072>(vb); ve[2] = tr_read<3080>(vb);
    va[3] = tr_read<3072>(vb); vc[3] = tr_read<3080>(vb); vd[3] = tr_read<4096>(vb); ve[3] = tr_read<4104>(vb);
    const int k0 = 4 * g + (rr & 1);
    unsigned par = 0u - (unsigned)(rr & 1), c0 = (unsigned)((k0 - rr - 1) >> 31), c1 = (unsigned)((k0 + 1 - rr) >> 31);
    asm volatile("" : "+v"(par), "+v"(c0), "+v"(c1));
    const float scale = 0.17677669529663687f;
    float e0[4], e1[4], mx[4], sm[4];
#pragma unroll
    for (int mi = 0; mi < 4; ++mi) {
      const float a0 = sel(c0, sel(par, s1[mi][1], s1[mi][0]), sel(par, s0[mi][1], s0[mi][0]));
      const float a1 = sel(c1, sel(par, s1[mi][3], s1[mi][2]), sel(par, s0[mi][3], s0[mi][2]));
      e0[mi] = rbf(rbf(a0) * scale); e1[mi] = rbf(rbf(a1) * scale);
      if (mi == 0) { const unsigned ok = mb0 == 0 ? 0u : ~0u; e0[0] = sel(c0 | ok, e0[0], -1e9f); e1[0] = sel(c1 | ok, e1[0], -1e9f); }
      mx[mi] = fmaxf(e0[mi], e1[mi]);
    }
#pragma unroll
    for (int mi = 0; mi < 4; ++mi) mx[mi] = red16<true>(mx[mi]);
#pragma unroll
    for (int mi = 0; mi < 4; ++mi) mx[mi] = red32<true>(mx[mi]);
#pragma unroll
    for (int mi = 0; mi < 4; ++mi) { e0[mi] = __expf(e0[mi] - mx[mi]); e1[mi] = __expf(e1[mi] - mx[mi]); sm[mi] = e0[mi] + e1[mi]; }
#pragma unroll
    for (int mi = 0; mi < 4; ++mi) sm[mi] = red16<false>(sm[mi]);
#pragma unroll
    for (int mi = 0; mi < 4; ++mi) sm[mi] = red32<false>(sm[mi]);
    WAIT_L0(); SCHED();
    char* dst = acat + (size_t)((mb0 * KB2 + j) * 1024) + lane16;
#pragma unroll
    for (int mi = 0; mi < 4; ++mi) {
      const float inv = __builtin_amdgcn_rcpf(sm[mi]);
      const unsigned pr = pk2(e0[mi] * inv, e1[mi] * inv);
      const unsigned hm = par ^ 0xffffu;
      const unsigned p0 = ((pr << 16) | (pr & 0xffffu)) & hm, p1 = ((pr & 0xffff0000u) | (pr >> 16)) & hm;
      const u32x4 pp = {p0 & ~c0, p1 & ~c1, p0 & c0, p1 & c1};
      const s16x8 pf8 = __builtin_bit_cast(s16x8, pp);
      const f32x4 o0 = MFMA(PK8(va[mi], vd[mi]), pf8, z);
      const f32x4 o1 = MFMA(PK8(vc[mi], ve[mi]), pf8, z);
      const u32x4 o = {pk2(o0[0], o0[1]), pk2(o0[2], o0[3]), pk2(o1[0], o1[1]), pk2(o1[2], o1[3])};
      st128(dst + mi * KB2 * 1024, o);
    }
  }
  ST(0, 4);
  ST(0, 5); ST(0, 6); ST(0, 8); ST(0, 7);
}

#define G2NST 5
constexpr int G2_NST = G2NST, G2_STG = 32768, G2_LDS = G2_NST * G2_STG, G2_NK = 36, G2_PFD = G2_NST + 8;
typedef __attribute__((address_space(1))) unsigned gu32;

__global__ __launch_bounds__(768, 3) void gemm2_kernel(const char* __restrict__ acat, const char* __restrict__ wo_,
                                                       const char* __restrict__ w2_, const char* __restrict__ x,
                                                       char* __restrict__ out, char* __restrict__ exch,
                                                       unsigned* __restrict__ flags) {
  extern __shared__ __attribute__((aligned(16))) char shm_[];
  lchar* shm = (lchar*)shm_;
  ST(1, 0); ST(1, 1);
  const int xcd = blockIdx.x & 7, idx = blockIdx.x >> 3;
  const int ks = xcd & 1, mt = (xcd >> 1) * 4 + (idx & 3), nt = idx >> 2;
  const int wid = __builtin_amdgcn_readfirstlane(threadIdx.x >> 6), lane = threadIdx.x & 63;
  const int wl = wid & 3, G = wid >> 2;
  const unsigned lane16 = lane * 16;
  if (G == 2) {
    const char* A = uni(acat); const char* Wo = uni(wo_); const char* W2 = uni(w2_);
    const unsigned a0 = ((mt * 8 + 2 * wl) * KB2 + ks * 2 * G2_NK) * 1024;
    unsigned wo[4];
#pragma unroll
    for (int k = 0; k < 4; ++k) {
      const int r = wl * 16 + k * 4 + (lane >> 4), pos = lane & 15;
      wo[k] = (r ^ (((r >> 3) & 1) << 2)) * 2048 + (pos ^ (((r >> 3) & 1) | ((r & 3) << 2))) * 16 + nt * 256;
    }
    auto issue = [&](int s, int buf) {
      const int kt = ks * G2_NK + s;
      lchar* dst = shm + buf * G2_STG + wl * 4096;
      const char* Wr = kt < 16 ? Wo : W2;
      const unsigned so = (kt < 16 ? kt : kt - 16) * 131072;
#pragma unroll
      for (int k = 0; k < 4; ++k) {
        glds(A, lane16, a0 + s * 2048 + (k >> 1) * (KB2 * 1024) + (k & 1) * 1024, dst + k * 1024);
        glds(Wr, wo[k], so, dst + 16384 + k * 1024);
      }
    };
    issue(0, 0); issue(1, 1); issue(2, 2);
    WAIT_V(16); BAR();
    issue(3, 3);
    WAIT_V(16); BAR();
    int ib = 4;
    for (int j = 0; j < G2_NK; ++j) {
      if (j + 4 < G2_NK) { issue(j + 4, ib); WAIT_V(16); }
      else if (j + 3 < G2_NK) WAIT_V(8); else WAIT_V(0);
      BAR();
      ib = ib + 1 == G2_NST ? 0 : ib + 1;
    }
    BAR();
    return;
  }
  const int wr = wl >> 1, wc = wl & 1;
  const int g = lane >> 4, q = (lane >> 2) & 3, p = lane & 3;
  const unsigned l0 = (unsigned)(size_t)shm;
  unsigned ab[4], wb[4];
#pragma unroll
  for (int mi = 0; mi < 4; ++mi) ab[mi] = l0 + wr * 8192 + (mi ^ (2 * G + ks)) * 2048 + lane16;
#pragma unroll
  for (int n = 0; n < 4; ++n)
    wb[n] = l0 + 16384 + ((8 * g + q) * 16 + 4 * ((wc * 2 + (n >> 1)) ^ q) + (p ^ (g & 1))) * 16 + (((p & 1) ^ (n & 1)) * 8);
  const int row = mt * 128 + wr * 64 + (2 * G + ks) * 16 + (lane & 15), tile = mt * 8 + nt;
  const size_t ooff = (size_t)row * 2048 + (nt * 128 + wc * 64 + g * 8) * 2;
  unsigned pfd = 0;
  {
    const char* a = g == 0 ? out + ooff : g == 1 ? x + ooff : g == 2 ? exch + (size_t)tile * 65536 + (lane & 1) * 32768 + wid * 4096
                                                                   : (const char*)(flags + ((tile * 2 + (lane & 1)) * 8 + wid) * 32);
    asm volatile("global_load_dword %0, %1, off sc1" : "+v"(pfd) : "v"(a) : "memory");
  }
  f32x4 acc[4][4];
#pragma unroll
  for (int mi = 0; mi < 4; ++mi)
#pragma unroll
    for (int n = 0; n < 4; ++n) { acc[mi][n] = (f32x4){0.f, 0.f, 0.f, 0.f}; asm volatile("" : "+v"(acc[mi][n])); }
  SCHED();
  BAR();
  ST(1, 2);
#define PH(a_, b_)
  if (G == 1) BAR();
  SCHED();
  unsigned bo = G * G2_STG;
  for (int s = G; s < G2_NK; s += 2) {
    s16x8 a[4][2]; s16x4 l[2][4], h[2][4];
    unsigned pa[4], pw[4];
#pragma unroll
    for (int i = 0; i < 4; ++i) { pa[i] = ab[i] + bo; pw[i] = wb[i] + bo; }
#pragma unroll
    for (int i = 0; i < 4; ++i) { a[i][0] = rd128<0>(pa[i]); l[0][i] = tr_read<0>(pw[i]); h[0][i] = tr_read<1024>(pw[i]); }
#pragma unroll
    for (int i = 0; i < 4; ++i) { a[i][1] = rd128<1024>(pa[i]); l[1][i] = tr_read<8192>(pw[i]); h[1][i] = tr_read<9216>(pw[i]); }
    WAIT_L(12); BAR(); SCHED();
    __builtin_amdgcn_s_setprio(1);
#pragma unroll
    for (int kh = 0; kh < 2; ++kh) {
      if (kh) { SCHED(); WAIT_L0(); SCHED(); }
#pragma unroll
      for (int n = 0; n < 4; ++n) {
        const s16x8 w = PK8(l[kh][n], h[kh][n]);
#pragma unroll
        for (int mi = 0; mi < 4; ++mi) acc[mi][n] = MFMA(w, a[mi][kh], acc[mi][n]);
      }
    }
    __builtin_amdgcn_s_setprio(0);
    SCHED(); BAR(); SCHED();
    bo += 2 * G2_STG; if (bo >= G2_NST * G2_STG) bo -= G2_NST * G2_STG;
  }
  if (G == 0) BAR();
  SCHED();
  ST(1, 3);
  WAIT_V(0);
  asm volatile("" :: "v"(pfd));
  const u32x4 xv0 = *(const u32x4*)(x + ooff), xv1 = *(const u32x4*)(x + ooff + 64);
#pragma unroll
  for (int i = 0; i < 2; ++i)
#pragma unroll
    for (int n = 0; n < 4; ++n) *(lf4*)(shm + ((wid * 2 + i) * 4 + n) * 1024 + lane16) = acc[2 + i][n];
  WAIT_L0(); BAR(); SCHED();
  f32x4 keep[4], send[4];
#pragma unroll
  for (int n = 0; n < 4; ++n) {
    send[n] = acc[1][n] + *(lf4*)(shm + (((wid ^ 4) * 2 + 1) * 4 + n) * 1024 + lane16);
    keep[n] = acc[0][n] + *(lf4*)(shm + (((wid ^ 4) * 2 + 0) * 4 + n) * 1024 + lane16);
  }
  const rsrc_t rs = mkrs(exch + (size_t)tile * 65536, 65536);
#pragma unroll
  for (int n = 0; n < 4; ++n)
    __builtin_amdgcn_raw_buffer_store_b128(__builtin_bit_cast(u32x4, send[n]), rs, ks * 32768 + (wid * 4 + n) * 1024 + lane16, 0, 16);
  ST(1, 4);
  WAIT_V(0);
  ST(1, 5);
  if (lane == 0) {
    __hip_atomic_store((gu32*)(flags + ((tile * 2 + ks) * 8 + wid) * 32), 1u, __ATOMIC_RELAXED, __HIP_MEMORY_SCOPE_AGENT);
    while (__hip_atomic_load((gu32*)(flags + ((tile * 2 + (ks ^ 1)) * 8 + wid) * 32), __ATOMIC_RELAXED, __HIP_MEMORY_SCOPE_AGENT) == 0u) __builtin_amdgcn_s_sleep(1);
  }
  ST(1, 6);
  asm volatile("" ::: "memory");
#pragma unroll
  for (int n = 0; n < 4; ++n)
    keep[n] += __builtin_bit_cast(f32x4, __builtin_amdgcn_raw_buffer_load_b128(rs, (ks ^ 1) * 32768 + (wid * 4 + n) * 1024 + lane16, 0, 16));
  const bool odd = g & 1;
#pragma unroll
  for (int u = 0; u < 2; ++u) {
    f32x4 lo, hi;
#pragma unroll
    for (int e = 0; e < 4; ++e) { lo[e] = odd ? keep[2 * u + 1][e] : keep[2 * u][e]; hi[e] = odd ? keep[2 * u][e] : keep[2 * u + 1][e]; }
    const u32x4 xv = u ? xv1 : xv0;
    const u32x4 o = {pk2(lo[0] + lo16(xv[0]), lo[1] + hi16(xv[0])), pk2(lo[2] + lo16(xv[1]), lo[3] + hi16(xv[1])),
                     pk2(hi[0] + lo16(xv[2]), hi[1] + hi16(xv[2])), pk2(hi[2] + lo16(xv[3]), hi[3] + hi16(xv[3]))};
    *(u32x4*)(out + ooff + u * 64) = o;
  }
  ST(1, 8); ST(1, 7);
}

extern "C" void kernel_launch(void* const* d_in, const int* in_sizes, int n_in, void* d_out, int out_size,
                              void* d_ws, size_t ws_size, hipStream_t stream) {
  const char* x = (const char*)d_in[0];
  const char* wqkv = (const char*)d_in[1];
  const char* wo = (const char*)d_in[2];
  const char* w1 = (const char*)d_in[4];
  const char* w2 = (const char*)d_in[5];
  char* ws = (char*)d_ws;
  (void)hipFuncSetAttribute((const void*)gemm1_kernel, hipFuncAttributeMaxDynamicSharedMemorySize, G1_LDS);
  (void)hipFuncSetAttribute((const void*)gemm2_kernel, hipFuncAttributeMaxDynamicSharedMemorySize, G2_LDS);
  gemm1_kernel<<<256, 512, G1_LDS, stream>>>(x, wqkv, w1, ws + WS_ACAT, wo, w2, (unsigned*)(ws + WS_FLAG));
  gemm2_kernel<<<256, 768, G2_LDS, stream>>>(ws + WS_ACAT, wo, w2, x, (char*)d_out, ws + WS_EXCH, (unsigned*)(ws + WS_FLAG));
}
```

```cpp
#include <hip/hip_runtime.h>
#include <hip/hip_bf16.h>

typedef short s16x4 __attribute__((ext_vector_type(4)));
typedef short s16x8 __attribute__((ext_vector_type(8)));
typedef __bf16 bf16x2 __attribute__((ext_vector_type(2)));
typedef float f32x4 __attribute__((ext_vector_type(4)));
typedef unsigned u32x4 __attribute__((ext_vector_type(4)));
typedef unsigned u32x2 __attribute__((ext_vector_type(2)));
typedef __attribute__((address_space(3))) char lchar;
typedef __attribute__((address_space(3))) unsigned lu32;

template <int V> struct IC { static constexpr int value = V; };
#define BAR() __builtin_amdgcn_s_barrier()
#define SCHED() __builtin_amdgcn_sched_barrier(0)
#define WAIT_V(n) asm volatile("s_waitcnt vmcnt(%0)" ::"n"(n) : "memory")
#define WAIT_L(n) asm volatile("s_waitcnt lgkmcnt(%0)" ::"n"(n) : "memory")
#define WAIT_L0() asm volatile("s_waitcnt lgkmcnt(0)" ::: "memory")
#define PK8(L, H) (s16x8){L[0], L[1], L[2], L[3], H[0], H[1], H[2], H[3]}
#define MFMA(a, b, c) __builtin_amdgcn_mfma_f32_16x16x32_bf16(a, b, c, 0, 0, 0)

#define ST(k, i)
constexpr int SEQ = 2048, DM = 1024, DFF = 3584, KC = 4608, KB2 = KC / 32;
constexpr size_t WS_XN = 0, WS_ACAT = 4u << 20, WS_EXCH = 24u << 20, WS_FLAG = 40u << 20;

__device__ __forceinline__ unsigned pk2(float a, float b) {
  bf16x2 v = {(__bf16)a, (__bf16)b};
  return __builtin_bit_cast(unsigned, v);
}
__device__ __forceinline__ float rbf(float a) { return (float)(__bf16)a; }
__device__ __forceinline__ float lo16(unsigned u) { return __builtin_bit_cast(float, u << 16); }
__device__ __forceinline__ float hi16(unsigned u) { return __builtin_bit_cast(float, u & 0xffff0000u); }

typedef short s16x2 __attribute__((ext_vector_type(2)));
__device__ __forceinline__ unsigned relu2(unsigned u) {
  const s16x2 z = {0, 0};
  asm("" : "+v"(u));
  return __builtin_bit_cast(unsigned, __builtin_elementwise_max(__builtin_bit_cast(s16x2, u), z));
}
template <bool MAX> __device__ __forceinline__ float red16(float v) {
  const unsigned u = __builtin_bit_cast(unsigned, v);
  auto r = __builtin_amdgcn_permlane16_swap(u, u, false, false);
  const unsigned r0 = r[0], r1 = r[1];
  const float a = __builtin_bit_cast(float, r0), b = __builtin_bit_cast(float, r1);
  return MAX ? fmaxf(a, b) : a + b;
}
template <bool MAX> __device__ __forceinline__ float red32(float v) {
  const unsigned u = __builtin_bit_cast(unsigned, v);
  auto r = __builtin_amdgcn_permlane32_swap(u, u, false, false);
  const unsigned r0 = r[0], r1 = r[1];
  const float a = __builtin_bit_cast(float, r0), b = __builtin_bit_cast(float, r1);
  return MAX ? fmaxf(a, b) : a + b;
}
template <int OFF> __device__ __forceinline__ s16x4 tr_read(unsigned a) {
  s16x4 r; asm volatile("ds_read_b64_tr_b16 %0, %1 offset:%2" : "=&v"(r) : "v"(a), "i"(OFF) : "memory"); return r;
}
template <int OFF> __device__ __forceinline__ s16x8 rd128(unsigned a) {
  s16x8 r; asm volatile("ds_read_b128 %0, %1 offset:%2" : "=&v"(r) : "v"(a), "i"(OFF) : "memory"); return r;
}
typedef __amdgpu_buffer_rsrc_t rsrc_t;
__device__ __forceinline__ rsrc_t mkrs(const void* p, unsigned bytes) {
  return __builtin_amdgcn_make_buffer_rsrc((void*)p, 0, bytes, 0x00020000);
}
__device__ __forceinline__ const char* uni(const char* p) {
  const unsigned long long u = (unsigned long long)p;
  const unsigned lo = __builtin_amdgcn_readfirstlane((unsigned)u), hi = __builtin_amdgcn_readfirstlane((unsigned)(u >> 32));
  return (const char*)(((unsigned long long)hi << 32) | lo);
}
#define NX_ 4
#define NQ_ 3
#define NW_ 4
#define PFL 15
#define PFR 13
#define EPRIO 0
#define STPOL "sc1"
__device__ __forceinline__ void st128(char* p, u32x4 v) { asm volatile("global_store_dwordx4 %0, %1, off " STPOL "\n\ts_nop 1" :: "v"(p), "v"(v) : "memory"); }
__device__ __forceinline__ void st64(char* p, u32x2 v) { asm volatile("global_store_dwordx2 %0, %1, off " STPOL "\n\ts_nop 1" :: "v"(p), "v"(v) : "memory"); }
__device__ __forceinline__ void glds(const char* base, unsigned voff, unsigned soff, lchar* l) {
  asm volatile("s_mov_b32 m0, %2\n\ts_nop 0\n\tglobal_load_lds_dwordx4 %0, %1"
               :: "v"(voff), "s"(base + soff), "s"((unsigned)(size_t)l) : "memory");
}

constexpr int G1_XU = 34816, G1_WH = 3 * G1_XU, G1_WQ = G1_WH + 32768, G1_ONE = G1_WQ + 24576, G1_LDS = G1_ONE + 16, G1_NT = 16;
typedef __attribute__((address_space(3))) u32x4 lu4;
typedef __attribute__((address_space(3))) u32x2 lu2;
typedef __attribute__((address_space(3))) f32x4 lf4;
typedef float f32x2 __attribute__((ext_vector_type(2)));
typedef __attribute__((address_space(3))) f32x2 lf2;
typedef __attribute__((address_space(3))) float lf1;

#define MM "v_mfma_f32_16x16x32_bf16 "
#define AMMA(c, a, b) asm volatile(MM "%0, %1, %2, %0" : "+v"(c) : "v"(a), "v"(b))
#define MMA_DONE() asm volatile("s_nop 15\n\ts_nop 15" ::: "memory")
__device__ __forceinline__ float sel(unsigned m, float a, float b) {
  return __builtin_bit_cast(float, (__builtin_bit_cast(unsigned, a) & m) | (__builtin_bit_cast(unsigned, b) & ~m));
}
__device__ __forceinline__ float redg(float v) { return red32<false>(red16<false>(v)); }
__device__ __forceinline__ f32x2 ln_stat(float s, float q) {
  const float mean = s * (1.f / DM), var = fmaxf(q * (1.f / DM) - mean * mean, 0.f);
  return (f32x2){rbf(mean), __builtin_amdgcn_rcpf(rbf(__builtin_amdgcn_sqrtf(rbf(rbf(var) + 1e-5f))))};
}
__device__ __forceinline__ f32x4 nrm(f32x4 a, f32x4 cs, f32x2 st) { return (a - st[0] * cs) * st[1]; }

__global__ __launch_bounds__(512, 2) void gemm1_kernel(const char* __restrict__ x_, const char* __restrict__ wqkv_,
                                                       const char* __restrict__ w1_, char* __restrict__ acat,
                                                       const char* __restrict__ wo_, const char* __restrict__ w2_,
                                                       unsigned* __restrict__ flags) {
  extern __shared__ __attribute__((aligned(16))) char shm_[];
  lchar* shm = (lchar*)shm_;
  ST(0, 0); ST(0, 1);
  const int xcd = blockIdx.x & 7, idx = blockIdx.x >> 3;
  const int mt = (xcd & 1) * 4 + (idx & 3), j = (xcd >> 1) * 8 + (idx >> 2);
  const int wid = __builtin_amdgcn_readfirstlane(threadIdx.x >> 6), lane = threadIdx.x & 63;
  const int G = wid >> 2, R = wid & 3;
  const int g = lane >> 4, q = (lane >> 2) & 3, p = lane & 3, rr = lane & 15;
  const unsigned l0 = (unsigned)(size_t)shm, lane16 = lane * 16;
  const char* x = uni(x_);
#define ROT 0
  const int rot = ROT;
  auto tk = [&](int t) { return (t + rot) & (G1_NT - 1); };
  const unsigned xo0 = (lane >> 3) * 2048 + ((lane & 7) ^ g) * 16, xo1 = (lane >> 3) * 2048 + ((lane & 7) ^ (4 + g)) * 16;
  auto issueX = [&](int h, int t, int xbuf) {
#pragma unroll
    for (int a = 0; a < NX_; ++a)
      glds(x, (a & 1) ? xo1 : xo0, (mt * 256 + h * 128 + R * 32 + a * 8) * 2048 + tk(t) * 128, shm + xbuf + (h * 16 + R * 4 + a) * 1024);
  };
  const unsigned xr0 = l0 + (R * 64 + rr) * 128 + (g ^ (rr >> 1)) * 16, xr1 = xr0 ^ 64;
  f32x4 acc[4][7];
  const f32x4 z = {0.f, 0.f, 0.f, 0.f};
  f32x4 gs[2] = {z, z}, gq[2] = {z, z}, cs[2] = {z, z};
  s16x8 ones = {0x3f80, 0x3f80, 0x3f80, 0x3f80, 0x3f80, 0x3f80, 0x3f80, 0x3f80};
  asm volatile("" : "+v"(ones), "+v"(gs[0]), "+v"(gs[1]), "+v"(gq[0]), "+v"(gq[1]), "+v"(cs[0]), "+v"(cs[1]));
  lf2* stat = (lf2*)shm;
  lf1* csl = (lf1*)(shm + 2304);
  lf1* hst = (lf1*)(shm + 3584);
  auto diag = [&](f32x4 d) { const unsigned m1 = 0u - (rr & 1), m2 = 0u - ((rr >> 1) & 1); return sel(m2, sel(m1, d[3], d[2]), sel(m1, d[1], d[0])); };
  auto put_stats = [&]() {
    if (g == q) { stat[R * 64 + G * 32 + rr] = ln_stat(gs[0][0], diag(gq[0])); stat[R * 64 + G * 32 + 16 + rr] = ln_stat(gs[1][0], diag(gq[1])); }
  };
  const int fo = j & 1;
  const int mb0 = mt * 16 + R * 4;
  unsigned pfd = 0;

  if (G == 0) {
    const char* wq = uni(wqkv_);
    issueX(1, 0, 0);
    SCHED();
    unsigned wo[3];
#pragma unroll
    for (int k = 0; k < 3; ++k) {
      const int i = (3 * R + k) * 64 + lane, r = i / 12, ch = (i % 12) ^ ((r >> 3) & 1);
      wo[k] = r * 6144 + (ch >> 2) * 2048 + j * 64 + (ch & 3) * 16;
    }
    const int hrow = mt > 0 ? mt * 256 - 16 : 0;
    auto issueQ = [&](int t, int B) {
#pragma unroll
      for (int k = 0; k < NQ_; ++k) glds(wq, wo[k], tk(t) * 64 * 6144, shm + G1_WQ + B * 12288 + (3 * R + k) * 1024);
    };
    auto issueH = [&](int t, int xbuf) {
      if (R < 2) glds(x, R ? xo1 : xo0, (hrow + R * 8) * 2048 + tk(t) * 128, shm + xbuf + (32 + R) * 1024);
    };
    issueQ(0, 0); issueH(0, 0); issueH(1, G1_XU); issueX(1, 1, G1_XU);
    SCHED();
    if (threadIdx.x < 8) flags[(blockIdx.x * 8 + threadIdx.x) * 32] = 0u;
    unsigned wb[7];
#pragma unroll
    for (int n = 0; n < 7; ++n) {
      const int F = n < 6 ? ((n >> 1) + R) % 3 + fo : (fo ? 0 : 3), f = n < 6 ? (n & 1) : fo;
      wb[n] = l0 + G1_WH + ((8 * g + q) * 16 + 4 * (F ^ q) + (p ^ (g & 1))) * 16 + (((p & 1) ^ f) * 8);
    }
#pragma unroll
    for (int mi = 0; mi < 4; ++mi)
#pragma unroll
      for (int n = 0; n < 7; ++n) { acc[mi][n] = (f32x4){0.f, 0.f, 0.f, 0.f}; asm volatile("" : "+v"(acc[mi][n])); }
    SCHED();
    WAIT_V(7); BAR();
    ST(0, 2);
    SCHED();
    int xc = 0, xn2 = 2 * G1_XU;
    auto half = [&](auto Bc, auto Kc, int t) {
      constexpr int B = decltype(Bc)::value, KH = decltype(Kc)::value;
      s16x8 xa[4]; s16x4 l[7], h[7];
      const unsigned xp = (KH ? xr1 : xr0) + xc;
      xa[0] = rd128<0>(xp); xa[1] = rd128<2048>(xp); xa[2] = rd128<4096>(xp); xa[3] = rd128<6144>(xp);
#pragma unroll
      for (int n = 0; n < 7; ++n) { l[n] = tr_read<B * 16384 + KH * 8192>(wb[n]); h[n] = tr_read<B * 16384 + KH * 8192 + 1024>(wb[n]); }
      if (KH == 0) {
        if (t + 1 < G1_NT) { issueQ(t + 1, B ^ 1); if (t + 2 < G1_NT) issueH(t + 2, xn2); WAIT_V(7); } else WAIT_V(0);
      } else {
        if (t + 2 < G1_NT) { issueX(1, t + 2, xn2); WAIT_V(7); } else WAIT_V(3);
      }
      WAIT_L0(); BAR(); SCHED();
      __builtin_amdgcn_s_setprio(1);
      asm volatile("s_cmp_eq_u32 %6, 3\n\ts_cbranch_scc1 1f\n\t" MM "%0, %2, %5, %0\n\t" MM "%1, %3, %5, %1\n\ts_branch 2f\n"
                   "1:\n\t" MM "%0, %4, %5, %0\n2:"
                   : "+v"(cs[0]), "+v"(cs[1]) : "v"(PK8(l[0], h[0])), "v"(PK8(l[1], h[1])), "v"(PK8(l[6], h[6])), "v"(ones), "s"(R) : "scc");
      SCHED();
#pragma unroll
      for (int n = 0; n < 7; ++n) {
        const s16x8 w = PK8(l[n], h[n]);
#pragma unroll
        for (int mi = 0; mi < 4; ++mi) AMMA(acc[mi][n], w, xa[mi]);
      }
#pragma unroll
      for (int i = 0; i < 2; ++i) { AMMA(gs[i], ones, xa[i]); AMMA(gq[i], xa[i], xa[i]); }
      __builtin_amdgcn_s_setprio(0);
      SCHED(); BAR(); SCHED();
    };
    for (int t = 0; t < G1_NT; t += 2) {
      half(IC<0>{}, IC<0>{}, t); half(IC<0>{}, IC<1>{}, t);
      xn2 = xc; xc = xc == 2 * G1_XU ? 0 : xc + G1_XU;
      half(IC<1>{}, IC<0>{}, t + 1); half(IC<1>{}, IC<1>{}, t + 1);
      xn2 = xc; xc = xc == 2 * G1_XU ? 0 : xc + G1_XU;
    }
    BAR(); MMA_DONE(); SCHED();
    ST(0, 3);
    put_stats();
    if (rr == 0) { *(lf4*)(csl + 2 * R * 16 + 4 * g) = cs[0]; if (R < 3) *(lf4*)(csl + (2 * R + 1) * 16 + 4 * g) = cs[1]; }
    __syncthreads();
    f32x4 cv[7];
#pragma unroll
    for (int n = 0; n < 7; ++n) cv[n] = *(lf4*)(csl + (n < 6 ? 2 * (((n >> 1) + R) % 3) + (n & 1) : 6) * 16 + 4 * g);
    char* dst = acat + (size_t)((mb0 * KB2 + 32 + 7 * (j >> 1)) * 1024) + lane16;
#pragma unroll
    for (int mi = 0; mi < 4; ++mi) {
      const f32x2 st = stat[R * 64 + mi * 16 + rr];
#pragma unroll
      for (int u = 0; u < 3; ++u) {
        const f32x4 a = nrm(acc[mi][2 * u], cv[2 * u], st), b = nrm(acc[mi][2 * u + 1], cv[2 * u + 1], st);
        const u32x4 o = {relu2(pk2(a[0], a[1])), relu2(pk2(a[2], a[3])), relu2(pk2(b[0], b[1])), relu2(pk2(b[2], b[3]))};
        st128(dst + (mi * KB2 + 4 * fo + (u + R) % 3) * 1024, o);
      }
      const f32x4 a = nrm(acc[mi][6], cv[6], st);
      const u32x2 o = {relu2(pk2(a[0], a[1])), relu2(pk2(a[2], a[3]))};
      st64(dst + (mi * KB2 + 3) * 1024 + fo * 8, o);
    }
  } else {
    const char* w1 = uni(w1_);
    issueX(0, 0, 0);
    unsigned wo[4];
#pragma unroll
    for (int k = 0; k < 4; ++k) {
      const int r = R * 16 + k * 4 + g;
      wo[k] = r * 7168 + (j >> 1) * 448 + (j & 1) * 192 + (rr ^ (((r >> 3) & 1) | ((r & 3) << 2))) * 16;
    }
    auto issueW = [&](int t, int B) {
#pragma unroll
      for (int k = 0; k < NW_; ++k) glds(w1, wo[k], tk(t) * 64 * 7168, shm + G1_WH + B * 16384 + (R * 4 + k) * 1024);
    };
    issueW(0, 0); issueX(0, 1, G1_XU); issueW(1, 1);
    SCHED();
    const unsigned lb = l0 + G1_WQ + ((8 * g + q) * 12 + (p ^ (g & 1))) * 16;
    const unsigned wb0 = lb + (p & 1) * 8, wb1 = wb0 ^ 8, wbh = lb + (1 + (R >> 1)) * 64 + (((p & 1) ^ (R & 1)) * 8);
    const unsigned hra = l0 + 32768 + rr * 128 + (g ^ (rr >> 1)) * 16;
    unsigned hr0 = rr ? hra : l0 + G1_ONE, hr1 = rr ? hra ^ 64 : l0 + G1_ONE, hm1 = rr ? 1u : 0u;
    asm volatile("" : "+v"(hr0), "+v"(hr1), "+v"(hm1));
    if (threadIdx.x == 256) *(lu4*)(shm + G1_ONE) = __builtin_bit_cast(u32x4, ones);
    const unsigned wu = blockIdx.x * 4 + R;
    const char* tp; unsigned ts;
    {
      const unsigned n = wu * 17 + min(lane, 16), s = 2 + (n >= 8704u), r = n >= 8704u ? n - 8704u : n;
      tp = r < 3072u ? wqkv_ + s * 393216u + r * 128u : r < 6656u ? w1_ + s * 458752u + (r - 3072u) * 128u : x_ + (r - 6656u) * 2048u + s * 128u;
      ts = r < 3072u ? 786432u : r < 6656u ? 917504u : 256u;
    }
    auto touch = [&](int t) {
      if (t == 7) {
        const unsigned n = min(wu * 11 + min(lane, 10), 10535u);
        tp = n < 8192u ? w2_ + n * 128u : wo_ + (n - 8192u) * 128u;
        ts = n < 8192u ? 8192u * 128u : 2340u * 128u;
      }
      if (lane < 17) asm volatile("global_load_dword %0, %1, off" : "+v"(pfd) : "v"(tp) : "memory");
      tp += ts;
    };
#pragma unroll
    for (int mi = 0; mi < 4; ++mi)
#pragma unroll
      for (int n = 0; n < 6; ++n) { acc[mi][n] = (f32x4){0.f, 0.f, 0.f, 0.f}; asm volatile("" : "+v"(acc[mi][n])); }
    f32x4 hacc = z; asm volatile("" : "+v"(hacc));
    SCHED();
    WAIT_V(8); WAIT_L0(); BAR();
    ST(0, 2);
    BAR();
    SCHED();
    int xc = 0, xn2 = 2 * G1_XU;
    auto half = [&](auto Bc, auto Kc, int t) {
      constexpr int B = decltype(Bc)::value, KH = decltype(Kc)::value, WO = B * 12288 + KH * 6144;
      s16x8 xa[4], hx; s16x4 l[6], h[6], hl, hh;
      const unsigned xp = (KH ? xr1 : xr0) + xc;
      xa[0] = rd128<0>(xp); xa[1] = rd128<2048>(xp); xa[2] = rd128<4096>(xp); xa[3] = rd128<6144>(xp);
      l[0] = tr_read<WO>(wb0); h[0] = tr_read<WO + 768>(wb0); l[1] = tr_read<WO>(wb1); h[1] = tr_read<WO + 768>(wb1);
      l[2] = tr_read<WO + 64>(wb0); h[2] = tr_read<WO + 832>(wb0); l[3] = tr_read<WO + 64>(wb1); h[3] = tr_read<WO + 832>(wb1);
      l[4] = tr_read<WO + 128>(wb0); h[4] = tr_read<WO + 896>(wb0); l[5] = tr_read<WO + 128>(wb1); h[5] = tr_read<WO + 896>(wb1);
      hl = tr_read<WO>(wbh); hh = tr_read<WO + 768>(wbh);
      hx = rd128<0>(__umul24(hm1, xc) + (KH ? hr1 : hr0));
      if (KH == 0) {
        if (t + 2 < G1_NT) {
          issueX(0, t + 2, xn2);
          touch(t);
        }
      } else {
        if (t + 2 < G1_NT) { issueW(t + 2, B); WAIT_V(9); } else WAIT_V(0);
      }
      WAIT_L0(); BAR(); SCHED();
      __builtin_amdgcn_s_setprio(1);
      asm volatile("s_cmp_gt_u32 %5, 1\n\ts_cbranch_scc1 1f\n\ts_cmp_eq_u32 %5, 1\n\ts_cbranch_scc1 2f\n\t" MM "%0, %1, %4, %0\n\ts_branch 3f\n"
                   "1:\n\t" MM "%0, %3, %3, %0\n\ts_branch 3f\n2:\n\t" MM "%0, %2, %4, %0\n3:"
                   : "+v"(cs[0]) : "v"(PK8(l[0], h[0])), "v"(PK8(l[1], h[1])), "v"(hx), "v"(ones), "s"(R) : "scc");
      SCHED();
#pragma unroll
      for (int n = 0; n < 6; ++n) {
        const s16x8 w = PK8(l[n], h[n]);
#pragma unroll
        for (int mi = 0; mi < 4; ++mi) AMMA(acc[mi][n], w, xa[mi]);
      }
      AMMA(hacc, PK8(hl, hh), hx);
#pragma unroll
      for (int i = 0; i < 2; ++i) { AMMA(gs[i], ones, xa[2 + i]); AMMA(gq[i], xa[2 + i], xa[2 + i]); }
      __builtin_amdgcn_s_setprio(0);
      SCHED(); BAR(); SCHED();
    };
    for (int t = 0; t < G1_NT; t += 2) {
      half(IC<0>{}, IC<0>{}, t); half(IC<0>{}, IC<1>{}, t);
      xn2 = xc; xc = xc == 2 * G1_XU ? 0 : xc + G1_XU;
      half(IC<1>{}, IC<0>{}, t + 1); half(IC<1>{}, IC<1>{}, t + 1);
      xn2 = xc; xc = xc == 2 * G1_XU ? 0 : xc + G1_XU;
    }
    asm volatile("" :: "v"(pfd));
    MMA_DONE(); SCHED();
    __builtin_amdgcn_s_setprio(EPRIO);
    ST(0, 3);
    if (threadIdx.x == 256) *(lu32*)(shm + 3712) = 0u;
    put_stats();
    if (rr == 0) { *(lf4*)(csl + (9 + R) * 16 + 4 * g) = hacc; if (R < 2) *(lf4*)(csl + (7 + R) * 16 + 4 * g) = cs[0]; }
    if (R == 2) { if (g == 0) hst[rr] = cs[0][0]; if (g == q) hst[16 + rr] = diag(cs[0]); }
    __syncthreads();
    f32x4 cv[6];
#pragma unroll
    for (int n = 0; n < 6; ++n) cv[n] = *(lf4*)(csl + (7 + n) * 16 + 4 * g);
    u32x4 fr[4][3];
    f32x2 st[4];
#pragma unroll
    for (int mi = 0; mi < 4; ++mi) st[mi] = stat[R * 64 + mi * 16 + rr];
    auto mkfr = [&](int mi, int u) {
      const f32x4 a = nrm(acc[mi][2 * u], cv[2 * u], st[mi]), c = nrm(acc[mi][2 * u + 1], cv[2 * u + 1], st[mi]);
      fr[mi][u] = (u32x4){pk2(a[0], a[1]), pk2(a[2], a[3]), pk2(c[0], c[1]), pk2(c[2], c[3])};
    };
    mkfr(3, 1);
#pragma unroll
    for (int mi = 0; mi < 4; ++mi) mkfr(mi, 2);
    lchar* vimg = shm + 4096;
    lchar* kblk = shm + 21504;
#pragma unroll
    for (int mi = 0; mi < 4; ++mi) *(lu4*)(vimg + (16 + R * 64 + mi * 16 + rr) * 64 + g * 16) = fr[mi][2];
    if (R < 3) *(lu4*)(kblk + (R + 1) * 1024 + lane16) = fr[3][1];
    {
      const f32x4 a = nrm(hacc, *(lf4*)(csl + (9 + R) * 16 + 4 * g), ln_stat(hst[rr], hst[16 + rr]));
      lchar* d = ((R < 2) ? (kblk + lane16) : (vimg + rr * 64 + g * 16)) + (R & 1) * 8;
      *(lu2*)d = (u32x2){pk2(a[0], a[1]), pk2(a[2], a[3])};
    }
    WAIT_L0();
    if (lane == 0) __hip_atomic_fetch_add((lu32*)(shm + 3712), 1u, __ATOMIC_RELAXED, __HIP_MEMORY_SCOPE_WORKGROUP);
    SCHED();
#pragma unroll
    for (int mi = 0; mi < 4; ++mi) { mkfr(mi, 0); if (mi < 3) mkfr(mi, 1); }
    f32x4 s0[4], s1[4];
#pragma unroll
    for (int mi = 0; mi < 4; ++mi) {
      s1[mi] = MFMA(__builtin_bit_cast(s16x8, fr[mi][1]), __builtin_bit_cast(s16x8, fr[mi][0]), z);
      if (mi) s0[mi] = MFMA(__builtin_bit_cast(s16x8, fr[mi - 1][1]), __builtin_bit_cast(s16x8, fr[mi][0]), z);
    }
    SCHED();
    if (lane == 0) {
      while (__hip_atomic_load((lu32*)(shm + 3712), __ATOMIC_RELAXED, __HIP_MEMORY_SCOPE_WORKGROUP) < 4u) __builtin_amdgcn_s_sleep(1);
    }
    asm volatile("" ::: "memory");
    SCHED();
    const u32x4 kprev = *(lu4*)(kblk + R * 1024 + lane16);
    s0[0] = MFMA(__builtin_bit_cast(s16x8, kprev), __builtin_bit_cast(s16x8, fr[0][0]), z);
    const unsigned vb = l0 + 4096 + (R * 64 + 4 * g + q) * 64 + p * 16;
    s16x4 va[4], vc[4], vd[4], ve[4];
    va[0] = tr_read<0>(vb); vc[0] = tr_read<8>(vb); vd[0] = tr_read<1024>(vb); ve[0] = tr_read<1032>(vb);
    va[1] = tr_read<1024>(vb); vc[1] = tr_read<1032>(vb); vd[1] = tr_read<2048>(vb); ve[1] = tr_read<2056>(vb);
    va[2] = tr_read<2048>(vb); vc[2] = tr_read<2056>(vb); vd[2] = tr_read<3072>(vb); ve[2] = tr_read<3080>(vb);
    va[3] = tr_read<3072>(vb); vc[3] = tr_read<3080>(vb); vd[3] = tr_read<4096>(vb); ve[3] = tr_read<4104>(vb);
    const int k0 = 4 * g + (rr & 1);
    unsigned par = 0u - (unsigned)(rr & 1), c0 = (unsigned)((k0 - rr - 1) >> 31), c1 = (unsigned)((k0 + 1 - rr) >> 31);
    asm volatile("" : "+v"(par), "+v"(c0), "+v"(c1));
    const float scale = 0.17677669529663687f;
    float e0[4], e1[4], mx[4], sm[4];
#pragma unroll
    for (int mi = 0; mi < 4; ++mi) {
      const float a0 = sel(c0, sel(par, s1[mi][1], s1[mi][0]), sel(par, s0[mi][1], s0[mi][0]));
      const float a1 = sel(c1, sel(par, s1[mi][3], s1[mi][2]), sel(par, s0[mi][3], s0[mi][2]));
      e0[mi] = rbf(rbf(a0) * scale); e1[mi] = rbf(rbf(a1) * scale);
      if (mi == 0) { const unsigned ok = mb0 == 0 ? 0u : ~0u; e0[0] = sel(c0 | ok, e0[0], -1e9f); e1[0] = sel(c1 | ok, e1[0], -1e9f); }
      mx[mi] = fmaxf(e0[mi], e1[mi]);
    }
#pragma unroll
    for (int mi = 0; mi < 4; ++mi) mx[mi] = red16<true>(mx[mi]);
#pragma unroll
    for (int mi = 0; mi < 4; ++mi) mx[mi] = red32<true>(mx[mi]);
#pragma unroll
    for (int mi = 0; mi < 4; ++mi) { e0[mi] = __expf(e0[mi] - mx[mi]); e1[mi] = __expf(e1[mi] - mx[mi]); sm[mi] = e0[mi] + e1[mi]; }
#pragma unroll
    for (int mi = 0; mi < 4; ++mi) sm[mi] = red16<false>(sm[mi]);
#pragma unroll
    for (int mi = 0; mi < 4; ++mi) sm[mi] = red32<false>(sm[mi]);
    WAIT_L0(); SCHED();
    char* dst = acat + (size_t)((mb0 * KB2 + j) * 1024) + lane16;
#pragma unroll
    for (int mi = 0; mi < 4; ++mi) {
      const float inv = __builtin_amdgcn_rcpf(sm[mi]);
      const unsigned pr = pk2(e0[mi] * inv, e1[mi] * inv);
      const unsigned hm = par ^ 0xffffu;
      const unsigned p0 = ((pr << 16) | (pr & 0xffffu)) & hm, p1 = ((pr & 0xffff0000u) | (pr >> 16)) & hm;
      const u32x4 pp = {p0 & ~c0, p1 & ~c1, p0 & c0, p1 & c1};
      const s16x8 pf8 = __builtin_bit_cast(s16x8, pp);
      const f32x4 o0 = MFMA(PK8(va[mi], vd[mi]), pf8, z);
      const f32x4 o1 = MFMA(PK8(vc[mi], ve[mi]), pf8, z);
      const u32x4 o = {pk2(o0[0], o0[1]), pk2(o0[2], o0[3]), pk2(o1[0], o1[1]), pk2(o1[2], o1[3])};
      st128(dst + mi * KB2 * 1024, o);
    }
  }
  ST(0, 4);
  ST(0, 5); ST(0, 6); ST(0, 8); ST(0, 7);
}

#define G2NST 5
constexpr int G2_NST = G2NST, G2_STG = 32768, G2_LDS = G2_NST * G2_STG, G2_NK = 36, G2_PFD = G2_NST + 8;
typedef __attribute__((address_space(1))) unsigned gu32;

__global__ __launch_bounds__(768, 3) void gemm2_kernel(const char* __restrict__ acat, const char* __restrict__ wo_,
                                                       const char* __restrict__ w2_, const char* __restrict__ x,
                                                       char* __restrict__ out, char* __restrict__ exch,
                                                       unsigned* __restrict__ flags) {
  extern __shared__ __attribute__((aligned(16))) char shm_[];
  lchar* shm = (lchar*)shm_;
  ST(1, 0); ST(1, 1);
  const int xcd = blockIdx.x & 7, idx = blockIdx.x >> 3;
  const int ks = xcd & 1, mt = (xcd >> 1) * 4 + (idx & 3), nt = idx >> 2;
  const int wid = __builtin_amdgcn_readfirstlane(threadIdx.x >> 6), lane = threadIdx.x & 63;
  const int wl = wid & 3, G = wid >> 2;
  const unsigned lane16 = lane * 16;
  if (G == 2) {
    const char* A = uni(acat); const char* Wo = uni(wo_); const char* W2 = uni(w2_);
    const unsigned a0 = ((mt * 8 + 2 * wl) * KB2 + ks * 2 * G2_NK) * 1024;
    unsigned wo[4];
#pragma unroll
    for (int k = 0; k < 4; ++k) {
      const int r = wl * 16 + k * 4 + (lane >> 4), pos = lane & 15;
      wo[k] = (r ^ (((r >> 3) & 1) << 2)) * 2048 + (pos ^ (((r >> 3) & 1) | ((r & 3) << 2))) * 16 + nt * 256;
    }
    auto issue = [&](int s, int buf) {
      const int kt = ks * G2_NK + s;
      lchar* dst = shm + buf * G2_STG + wl * 4096;
      const char* Wr = kt < 16 ? Wo : W2;
      const unsigned so = (kt < 16 ? kt : kt - 16) * 131072;
#pragma unroll
      for (int k = 0; k < 4; ++k) {
        glds(A, lane16, a0 + s * 2048 + (k >> 1) * (KB2 * 1024) + (k & 1) * 1024, dst + k * 1024);
        glds(Wr, wo[k], so, dst + 16384 + k * 1024);
      }
    };
    issue(0, 0); issue(1, 1); issue(2, 2);
    WAIT_V(16); BAR();
    issue(3, 3);
    WAIT_V(16); BAR();
    int ib = 4;
    for (int j = 0; j < G2_NK; ++j) {
      if (j + 4 < G2_NK) { issue(j + 4, ib); WAIT_V(16); }
      else if (j + 3 < G2_NK) WAIT_V(8); else WAIT_V(0);
      BAR();
      ib = ib + 1 == G2_NST ? 0 : ib + 1;
    }
    BAR();
    return;
  }
  const int wr = wl >> 1, wc = wl & 1;
  const int g = lane >> 4, q = (lane >> 2) & 3, p = lane & 3;
  const unsigned l0 = (unsigned)(size_t)shm;
  unsigned ab[4], wb[4];
#pragma unroll
  for (int mi = 0; mi < 4; ++mi) ab[mi] = l0 + wr * 8192 + (mi ^ (2 * G + ks)) * 2048 + lane16;
#pragma unroll
  for (int n = 0; n < 4; ++n)
    wb[n] = l0 + 16384 + ((8 * g + q) * 16 + 4 * ((wc * 2 + (n >> 1)) ^ q) + (p ^ (g & 1))) * 16 + (((p & 1) ^ (n & 1)) * 8);
  f32x4 acc[4][4];
#pragma unroll
  for (int mi = 0; mi < 4; ++mi)
#pragma unroll
    for (int n = 0; n < 4; ++n) { acc[mi][n] = (f32x4){0.f, 0.f, 0.f, 0.f}; asm volatile("" : "+v"(acc[mi][n])); }
  SCHED();
  BAR();
  ST(1, 2);
#define PH(a_, b_)
  if (G == 1) BAR();
  SCHED();
  unsigned bo = G * G2_STG;
  for (int s = G; s < G2_NK; s += 2) {
    s16x8 a[4][2]; s16x4 l[2][4], h[2][4];
    unsigned pa[4], pw[4];
#pragma unroll
    for (int i = 0; i < 4; ++i) { pa[i] = ab[i] + bo; pw[i] = wb[i] + bo; }
#pragma unroll
    for (int i = 0; i < 4; ++i) { a[i][0] = rd128<0>(pa[i]); l[0][i] = tr_read<0>(pw[i]); h[0][i] = tr_read<1024>(pw[i]); }
#pragma unroll
    for (int i = 0; i < 4; ++i) { a[i][1] = rd128<1024>(pa[i]); l[1][i] = tr_read<8192>(pw[i]); h[1][i] = tr_read<9216>(pw[i]); }
    WAIT_L(12); BAR(); SCHED();
    __builtin_amdgcn_s_setprio(1);
#pragma unroll
    for (int kh = 0; kh < 2; ++kh) {
      if (kh) { SCHED(); WAIT_L0(); SCHED(); }
#pragma unroll
      for (int n = 0; n < 4; ++n) {
        const s16x8 w = PK8(l[kh][n], h[kh][n]);
#pragma unroll
        for (int mi = 0; mi < 4; ++mi) acc[mi][n] = MFMA(w, a[mi][kh], acc[mi][n]);
      }
    }
    __builtin_amdgcn_s_setprio(0);
    SCHED(); BAR(); SCHED();
    bo += 2 * G2_STG; if (bo >= G2_NST * G2_STG) bo -= G2_NST * G2_STG;
  }
  if (G == 0) BAR();
  SCHED();
  ST(1, 3);
  const int row = mt * 128 + wr * 64 + (2 * G + ks) * 16 + (lane & 15);
  const size_t ooff = (size_t)row * 2048 + (nt * 128 + wc * 64 + g * 8) * 2;
  const u32x4 xv0 = *(const u32x4*)(x + ooff), xv1 = *(const u32x4*)(x + ooff + 64);
#pragma unroll
  for (int i = 0; i < 2; ++i)
#pragma unroll
    for (int n = 0; n < 4; ++n) *(lf4*)(shm + ((wid * 2 + i) * 4 + n) * 1024 + lane16) = acc[2 + i][n];
  WAIT_L0(); BAR(); SCHED();
  f32x4 keep[4], send[4];
#pragma unroll
  for (int n = 0; n < 4; ++n) {
    send[n] = acc[1][n] + *(lf4*)(shm + (((wid ^ 4) * 2 + 1) * 4 + n) * 1024 + lane16);
    keep[n] = acc[0][n] + *(lf4*)(shm + (((wid ^ 4) * 2 + 0) * 4 + n) * 1024 + lane16);
  }
  const int tile = mt * 8 + nt;
  const rsrc_t rs = mkrs(exch + (size_t)tile * 65536, 65536);
#pragma unroll
  for (int n = 0; n < 4; ++n)
    __builtin_amdgcn_raw_buffer_store_b128(__builtin_bit_cast(u32x4, send[n]), rs, ks * 32768 + (wid * 4 + n) * 1024 + lane16, 0, 16);
  ST(1, 4);
  WAIT_V(0);
  ST(1, 5);
  if (lane == 0) {
    __hip_atomic_store((gu32*)(flags + ((tile * 2 + ks) * 8 + wid) * 32), 1u, __ATOMIC_RELAXED, __HIP_MEMORY_SCOPE_AGENT);
    while (__hip_atomic_load((gu32*)(flags + ((tile * 2 + (ks ^ 1)) * 8 + wid) * 32), __ATOMIC_RELAXED, __HIP_MEMORY_SCOPE_AGENT) == 0u) __builtin_amdgcn_s_sleep(1);
  }
  ST(1, 6);
  asm volatile("" ::: "memory");
#pragma unroll
  for (int n = 0; n < 4; ++n)
    keep[n] += __builtin_bit_cast(f32x4, __builtin_amdgcn_raw_buffer_load_b128(rs, (ks ^ 1) * 32768 + (wid * 4 + n) * 1024 + lane16, 0, 16));
  const bool odd = g & 1;
#pragma unroll
  for (int u = 0; u < 2; ++u) {
    f32x4 lo, hi;
#pragma unroll
    for (int e = 0; e < 4; ++e) { lo[e] = odd ? keep[2 * u + 1][e] : keep[2 * u][e]; hi[e] = odd ? keep[2 * u][e] : keep[2 * u + 1][e]; }
    const u32x4 xv = u ? xv1 : xv0;
    const u32x4 o = {pk2(lo[0] + lo16(xv[0]), lo[1] + hi16(xv[0])), pk2(lo[2] + lo16(xv[1]), lo[3] + hi16(xv[1])),
                     pk2(hi[0] + lo16(xv[2]), hi[1] + hi16(xv[2])), pk2(hi[2] + lo16(xv[3]), hi[3] + hi16(xv[3]))};
    *(u32x4*)(out + ooff + u * 64) = o;
  }
  ST(1, 8); ST(1, 7);
}

extern "C" void kernel_launch(void* const* d_in, const int* in_sizes, int n_in, void* d_out, int out_size,
                              void* d_ws, size_t ws_size, hipStream_t stream) {
  const char* x = (const char*)d_in[0];
  const char* wqkv = (const char*)d_in[1];
  const char* wo = (const char*)d_in[2];
  const char* w1 = (const char*)d_in[4];
  const char* w2 = (const char*)d_in[5];
  char* ws = (char*)d_ws;
  (void)hipFuncSetAttribute((const void*)gemm1_kernel, hipFuncAttributeMaxDynamicSharedMemorySize, G1_LDS);
  (void)hipFuncSetAttribute((const void*)gemm2_kernel, hipFuncAttributeMaxDynamicSharedMemorySize, G2_LDS);
  gemm1_kernel<<<256, 512, G1_LDS, stream>>>(x, wqkv, w1, ws + WS_ACAT, wo, w2, (unsigned*)(ws + WS_FLAG));
  gemm2_kernel<<<256, 768, G2_LDS, stream>>>(ws + WS_ACAT, wo, w2, x, (char*)d_out, ws + WS_EXCH, (unsigned*)(ws + WS_FLAG));
}
```
